```python
import torch

def _softmax(x, axis=-1):
    return torch.softmax(x, dim=axis)

def _gelu(x):
    return torch.nn.functional.gelu(x, approximate='tanh')

def _silu(x):
    return torch.nn.functional.silu(x)

def _sigmoid(x):
    return torch.sigmoid(x)

def _layernorm(x, axis=-1, eps=1e-05):
    m = x.mean(dim=axis, keepdim=True)
    v = x.var(dim=axis, keepdim=True, unbiased=False)
    return (x - m) / torch.sqrt(v + eps)

def _scatter_add(tgt, idx, src, axis=0):
    return tgt.clone().index_add_(axis, idx, src)

def _segment_sum(src, seg, n):
    out = torch.zeros((n,) + src.shape[1:], dtype=src.dtype)
    return out.index_add_(0, seg, src)

def _pad(x, pw):
    flat = tuple((v for pair in reversed(pw) for v in pair))
    return torch.nn.functional.pad(x, flat)
kernel_name = 'standalone__tensor_product_equiv_mace__graph'

def setup_inputs():

    _w = lambda a: (lambda t: t.to(torch.bfloat16) if t.is_floating_point() else t)(torch.as_tensor(a))
    N, deg = (10000, 16)
    Lmax, C, De = (1, 256, 16)
    M = (Lmax + 1) ** 2
    E = N * deg
    src = (lambda lo, hi, *s: torch.randint(lo, hi, s))(0, N, E).to(torch.int64)
    dst = (lambda lo, hi, *s: torch.randint(lo, hi, s))(0, N, E).to(torch.int64)
    r = (lambda *s: torch.randn(*s, dtype=torch.bfloat16) * 0.05)(E, 3).to(float)
    r = r / (torch.linalg.norm(r, dim=-1, keepdim=True).to(float) + 1e-06)
    rx, ry, rz = (r[:, 0].clone(), r[:, 1].clone(), r[:, 2].clone())
    x2, y2, z2 = (rx * rx, ry * ry, rz * rz)
    sh_list = [torch.ones_like(rx), rx, ry, rz, rx * ry, ry * rz, 2.0 * z2 - x2 - y2, rx * rz, x2 - y2, ry * (3.0 * x2 - y2), rx * ry * rz, ry * (4.0 * z2 - x2 - y2), rz * (2.0 * z2 - 3.0 * x2 - 3.0 * y2), rx * (4.0 * z2 - x2 - y2), rz * (x2 - y2), rx * (x2 - 3.0 * y2)]
    sh16 = torch.stack(sh_list, dim=-1).to(float)
    pad = max(0, M - 16)
    sh_full = torch.cat([sh16, torch.zeros((E, pad), dtype=float)], dim=-1)[:, :M]
    cg = ((lambda *s: torch.randn(*s, dtype=torch.bfloat16) * 0.05)(M, M, M) * (1.0 / float(M))).to(float)
    return {'x': _w(((lambda *s: torch.randn(*s, dtype=torch.bfloat16) * 0.05)(N, M, C) * 0.3).to(float)), 'src': _w(src), 'dst': _w(dst), 'sh': _w(sh_full.to(float)), 'edge_attr': _w(((lambda *s: torch.randn(*s, dtype=torch.bfloat16) * 0.05)(E, De) * 0.3).to(float)), 'cg': _w(cg), 'w_r': _w(((lambda *s: torch.randn(*s, dtype=torch.bfloat16) * 0.05)(De, C) * (1.0 / float(De) ** 0.5)).to(float)), 'w_out': _w(((lambda *s: torch.randn(*s, dtype=torch.bfloat16) * 0.05)(C, C) * (1.0 / float(C) ** 0.5)).to(float))}

def reference(x, src, dst, sh, edge_attr, cg, w_r, w_out):
    N = x.shape[0]
    M = x.shape[1]
    C = x.shape[2]
    h = x[src]
    tp = torch.einsum('uvw,euc,ev->ewc', cg, h, sh)
    rw = edge_attr @ w_r
    msg = tp * rw[:, None, :]
    msg2 = msg.reshape(-1, M * C)
    agg = _segment_sum(msg2, dst, N).reshape(N, M, C)
    out = torch.einsum('nmc,cd->nmd', agg, w_out)
    return out
```

```cpp
#include <hip/hip_runtime.h>
#include <stdint.h>

typedef unsigned int u32;
typedef unsigned short u16;
typedef u32 __attribute__((ext_vector_type(4))) u32x4;
typedef u32 __attribute__((ext_vector_type(2))) u32x2;
typedef float __attribute__((ext_vector_type(4))) f32x4;
typedef float __attribute__((ext_vector_type(2))) f32x2;
typedef short __attribute__((ext_vector_type(4))) s16x4;
typedef __bf16 __attribute__((ext_vector_type(8))) bf16x8;
typedef __bf16 __attribute__((ext_vector_type(2))) bf16x2;

constexpr int NN = 10000, NE = 160000, HALF = 5000;
constexpr int NB = 313;
constexpr int NCH = 256, CH = 625;
constexpr int CAPC = 36;
constexpr int CELLS = NCH * CAPC;
constexpr int RSTR = CELLS + 1;
constexpr u32 SENT = 0xFFFFFFFFu, OVF = 0xFFFFFFFEu;
constexpr int ITCAP = 512;

constexpr size_t WS_META = 0;
constexpr size_t WS_WFRAG = WS_META + (size_t)64 * CELLS * 4;
constexpr size_t WS_REC = WS_WFRAG + 131072;
constexpr size_t WS_PART = WS_REC + (size_t)64 * RSTR * 64;
constexpr size_t WS_SINK = WS_PART + (size_t)2 * NN * 2048;

__device__ __forceinline__ u32 pack2(float a, float b) {
  f32x2 v = {a, b};
  bf16x2 r = __builtin_convertvector(v, bf16x2);
  return __builtin_bit_cast(u32, r);
}
__device__ __forceinline__ float bflo(u32 v) { return __uint_as_float(v << 16); }
__device__ __forceinline__ float bfhi(u32 v) { return __uint_as_float(v & 0xFFFF0000u); }
__device__ __forceinline__ float bf1(u16 v) { return __uint_as_float((u32)v << 16); }

__global__ __launch_bounds__(640) void k1(const u32x4* __restrict__ x, const int* __restrict__ src,
                                          const int* __restrict__ dst, const u32x2* __restrict__ sh,
                                          const u32x4* __restrict__ ea, const u32* __restrict__ cg,
                                          const u16* __restrict__ wout, u32* __restrict__ meta,
                                          u32x4* __restrict__ rec, u32x4* __restrict__ wfrag,
                                          u32* __restrict__ sink) {
  __shared__ u32 cnt[64];
  __shared__ u16 ids[64 * CAPC];
  const int t = threadIdx.x, c = blockIdx.x;
  const bool act = t < CH;
  const int e = c * CH + (act ? t : 0);
  const int d = dst[e], s = src[e];
  const u32x2 shv = sh[e];
  const u32x4 ea0 = ea[2 * e], ea1 = ea[2 * e + 1];
  if (t < 64) cnt[t] = 0;

  u32x4 pf[8];
  {
    const int role = c & 7, j = c >> 3;
    const u32x4* xb = x + (size_t)(role & 1) * HALF * 128 + (role >> 1) * 8 + (t & 7);
#pragma unroll
    for (int k = 0; k < 8; k++) {
      int i = (t >> 3) + 80 * k;
      i = j * 625 + (i < 625 ? i : 624);
      pf[k] = xb[(size_t)i * 32];
    }
  }
  if (t < 32) {
    const int item = c * 32 + t;
    const int tile = item >> 9, ks = (item >> 6) & 7, l = item & 63;
    const int rho = l & 15;
    const int dd = 32 * (tile >> 1) + 8 * (rho >> 2) + 4 * (tile & 1) + (rho & 3);
    const u16* wp = wout + (size_t)(32 * ks + 8 * (l >> 4)) * 256 + dd;
    u32x4 v;
    v.x = (u32)wp[0] | ((u32)wp[256] << 16);
    v.y = (u32)wp[512] | ((u32)wp[768] << 16);
    v.z = (u32)wp[1024] | ((u32)wp[1280] << 16);
    v.w = (u32)wp[1536] | ((u32)wp[1792] << 16);
    wfrag[item] = v;
  }
  if (c < 64 && t >= 64 && t < 68) {
    u32x4 z = {0, 0, 0, 0};
    rec[((size_t)c * RSTR + CELLS) * 4 + (t - 64)] = z;
  }
  __syncthreads();
  const int bin = d / NB, ld = d - bin * NB;
  const int hb = bin * 2 + (s >= HALF ? 1 : 0);
  if (act) {
    u32 rank = atomicAdd(&cnt[hb], 1u);
    if (rank < CAPC) ids[hb * CAPC + rank] = (u16)t;
  }
  u32x4 a0, a1;
  {
    const float s0 = bflo(shv.x), s1 = bfhi(shv.x), s2 = bflo(shv.y), s3 = bfhi(shv.y);
    float A[4][4];
#pragma unroll
    for (int u = 0; u < 4; u++) {
#pragma unroll
      for (int wp = 0; wp < 2; wp++) {
        const u32 c0 = cg[u * 8 + 0 + wp], c1 = cg[u * 8 + 2 + wp], c2 = cg[u * 8 + 4 + wp], c3 = cg[u * 8 + 6 + wp];
        A[2 * wp][u] = bflo(c0) * s0 + bflo(c1) * s1 + bflo(c2) * s2 + bflo(c3) * s3;
        A[2 * wp + 1][u] = bfhi(c0) * s0 + bfhi(c1) * s1 + bfhi(c2) * s2 + bfhi(c3) * s3;
      }
    }
    a0.x = pack2(A[0][0], A[0][1]); a0.y = pack2(A[0][2], A[0][3]);
    a0.z = pack2(A[1][0], A[1][1]); a0.w = pack2(A[1][2], A[1][3]);
    a1.x = pack2(A[2][0], A[2][1]); a1.y = pack2(A[2][2], A[2][3]);
    a1.z = pack2(A[3][0], A[3][1]); a1.w = pack2(A[3][2], A[3][3]);
  }
  __syncthreads();
  if (act) {
    const u32 n = cnt[hb];
    if (n <= CAPC) {
      u32 fr = 0;
      for (u32 i = 0; i < n; i++) fr += (ids[hb * CAPC + i] < (u16)t) ? 1u : 0u;
      const u32 ci = c * CAPC + fr;
      meta[(size_t)hb * CELLS + ci] = (u32)s | ((u32)ld << 14);
      u32x4* rp = rec + ((size_t)hb * RSTR + ci) * 4;
      rp[0] = a0; rp[1] = a1; rp[2] = ea0; rp[3] = ea1;
    }
  }
  for (int i = t; i < 64 * CAPC; i += 640) {
    const int h2 = i / CAPC, p = i - h2 * CAPC;
    const u32 n = cnt[h2];
    if (n > CAPC) meta[(size_t)h2 * CELLS + c * CAPC + p] = OVF;
    else if ((u32)p >= n) meta[(size_t)h2 * CELLS + c * CAPC + p] = SENT;
  }
  u32x4 z = pf[0];
#pragma unroll
  for (int k = 1; k < 8; k++) z ^= pf[k];
  const u32 zz = z.x ^ z.y ^ z.z ^ z.w;
  if (zz == 0x9E3779B9u && s == -1) sink[0] = zz;
}

struct K2Lds {
  u32 slot[ITCAP * 8];
  u32x4 stage[8][2][128];
  u32 deg[320];
  u32 dh[64];
  u32 posb[320];
  u16 sorted[320];
  u32 base[40];
  u32 blen[40];
  u32 wq[8][2];
  u32 flag;
  u32 wcnt[8];
};

__device__ __forceinline__ u32 slot_index(u32 it, u32 g) { return ((it >> 2) << 5) + (g << 2) + (it & 3); }

__global__ __launch_bounds__(512, 2) void k2(const u32x4* __restrict__ x, const u32* __restrict__ meta,
                                             const u32x4* __restrict__ rec, const u32x4* __restrict__ wr,
                                             u32x4* __restrict__ part, const int* __restrict__ src,
                                             const int* __restrict__ dst, const u16* __restrict__ sh,
                                             const u16* __restrict__ ea, const u16* __restrict__ cg) {
  __shared__ K2Lds L;
  const int t = threadIdx.x, b = blockIdx.x;
  const int lane = t & 63, wv = __builtin_amdgcn_readfirstlane(t >> 6);
  const int sl = (b & 7) >> 1, h = b & 1, bin = b >> 3;
  const int hb = bin * 2 + h;
  const int nnode = (bin == 31) ? (NN - 31 * NB) : NB;

  u32x4 m[5];
  {
    const u32x4* mp = (const u32x4*)(meta + (size_t)hb * CELLS);
#pragma unroll
    for (int k = 0; k < 4; k++) m[k] = mp[t + 512 * k];
    u32x4 sv = {SENT, SENT, SENT, SENT};
    m[4] = sv;
    if (t < 256) m[4] = mp[t + 2048];
  }
  u32x4 wrow[8];
  {
    const int q = lane >> 4, col = lane & 15;
    const u32x4* wp = wr + (size_t)(8 * (q & 1)) * 32 + sl * 8 + (col & 7);
#pragma unroll
    for (int jj = 0; jj < 8; jj++) wrow[jj] = wp[jj * 32];
  }
  if (t < 320) L.deg[t] = 0;
  if (t < 64) L.dh[t] = 0;
  if (t == 0) L.flag = 0;
  __syncthreads();
  u32 rk[5][4];
#pragma unroll
  for (int k = 0; k < 5; k++) {
#pragma unroll
    for (int i = 0; i < 4; i++) {
      const u32 v = m[k][i];
      rk[k][i] = 0;
      if (v < OVF) rk[k][i] = atomicAdd(&L.deg[v >> 14], 1u);
      else if (v == OVF) L.flag = 1;
    }
  }
  __syncthreads();
  u32 mydeg = 0, r2 = 0;
  if (t < 320) {
    mydeg = L.deg[t];
    r2 = atomicAdd(&L.dh[63 - (mydeg < 63 ? mydeg : 63)], 1u);
  }
  __syncthreads();
  if (wv == 0) {
    const u32 v = L.dh[lane];
    u32 inc = v;
#pragma unroll
    for (int o = 1; o < 64; o <<= 1) {
      const u32 nb = __shfl_up(inc, o);
      if (lane >= o) inc += nb;
    }
    L.dh[lane] = inc - v;
  }
  __syncthreads();
  if (t < 320) {
    const u32 p = L.dh[63 - (mydeg < 63 ? mydeg : 63)] + r2;
    L.posb[t] = p;
    L.sorted[p] = (u16)t;
  }
  __syncthreads();
  if (wv == 0) {
    const int w = lane >> 3, k = lane & 7;
    const int bt = 8 * k + ((k & 1) ? 7 - w : w);
    u32 len = 0;
    if (k < 5) {
#pragma unroll
      for (int g = 0; g < 8; g++) {
        const u32 dg = L.deg[L.sorted[bt * 8 + g]];
        len = dg > len ? dg : len;
      }
      if (len == 0) len = 1;
    }
    u32 tot = len;
#pragma unroll
    for (int o = 1; o < 8; o <<= 1) tot += __shfl_xor(tot, o);
    if (k == 5) len = (4 - (tot & 3)) & 3;
    u32 inc = len;
#pragma unroll
    for (int o = 1; o < 64; o <<= 1) {
      const u32 nb = __shfl_up(inc, o);
      if (lane >= o) inc += nb;
    }
    const u32 st = inc - len;
    if (k < 5) { L.base[bt] = st; L.blen[bt] = len; }
    if (k == 0) { L.wq[w][0] = st >> 2; L.wq[w][1] = (tot + 3) >> 2; }
    if (lane == 63 && inc > ITCAP) L.flag = 1;
    if (k == 5 && inc <= ITCAP) {
      for (u32 i = 0; i < len; i++)
#pragma unroll
        for (int g = 0; g < 8; g++) L.slot[slot_index(st + i, g)] = (u32)(h * HALF) | ((u32)CELLS << 14);
    }
  }
  __syncthreads();
  if (L.flag == 0) {
    u32 sb[5][4];
#pragma unroll
    for (int k = 0; k < 5; k++) {
#pragma unroll
      for (int i = 0; i < 4; i++) {
        const u32 v = m[k][i];
        sb[k][i] = 0;
        if (v < OVF) {
          const u32 p = L.posb[v >> 14];
          const u32 bs = L.base[p >> 3];
          sb[k][i] = bs | ((p & 7) << 16);
          const u32 flat = (u32)(t + 512 * k) * 4 + i;
          L.slot[slot_index(bs + rk[k][i], p & 7)] = (v & 0x3FFFu) | (flat << 14);
        }
      }
    }
    __syncthreads();
#pragma unroll
    for (int k = 0; k < 5; k++) {
#pragma unroll
      for (int i = 0; i < 4; i++) {
        const u32 v = m[k][i];
        if (v < OVF) {
          const u32 dg = L.deg[v >> 14];
          const u32 bs = sb[k][i] & 0xFFFFu, g = sb[k][i] >> 16;
          const u32 flat = (u32)(t + 512 * k) * 4 + i;
          u32 fr = 0;
          for (u32 r = 0; r < dg; r++) fr += ((L.slot[slot_index(bs + r, g)] >> 14) < flat) ? 1u : 0u;
          rk[k][i] = fr;
        }
      }
    }
    if (t < 320) {
      const u32 p = L.posb[t];
      const u32 bs = L.base[p >> 3], len = L.blen[p >> 3];
      u32 ps = (u32)(h * HALF);
      if (mydeg > 0) ps = L.slot[slot_index(bs, p & 7)] & 0x3FFFu;
      for (u32 r = mydeg; r < len; r++) L.slot[slot_index(bs + r, p & 7)] = ps | ((u32)CELLS << 14);
    }
    __syncthreads();
#pragma unroll
    for (int k = 0; k < 5; k++) {
#pragma unroll
      for (int i = 0; i < 4; i++) {
        const u32 v = m[k][i];
        if (v < OVF) {
          const u32 bs = sb[k][i] & 0xFFFFu, g = sb[k][i] >> 16;
          const u32 flat = (u32)(t + 512 * k) * 4 + i;
          L.slot[slot_index(bs + rk[k][i], g)] = (v & 0x3FFFu) | (flat << 14);
        }
      }
    }
    __syncthreads();

    const int g = lane >> 3, li = lane & 7;
    u32x4 Bj[8];
    {
      const bool nz = ((lane >> 5) == ((lane >> 3) & 1));
#pragma unroll
      for (int j = 0; j < 8; j++) {
#pragma unroll
        for (int p = 0; p < 4; p++) {
          const u32 lo = wrow[2 * p][j >> 1], hi = wrow[2 * p + 1][j >> 1];
          const u32 v = (j & 1) ? __builtin_amdgcn_perm(hi, lo, 0x07060302u) : __builtin_amdgcn_perm(hi, lo, 0x05040100u);
          Bj[j][p] = nz ? v : 0u;
        }
      }
    }
    const u32 q0 = __builtin_amdgcn_readfirstlane(L.wq[wv][0]);
    const int nq = __builtin_amdgcn_readfirstlane(L.wq[wv][1]);
    u32 te[5];
    u32 nd[5];
#pragma unroll
    for (int k = 0; k < 5; k++) {
      const int bt = 8 * k + ((k & 1) ? 7 - wv : wv);
      te[k] = __builtin_amdgcn_readfirstlane(L.base[bt] + L.blen[bt] - q0 * 4);
      const u32 n = L.sorted[bt * 8 + g];
      const u32 dg = L.deg[n];
      nd[k] = (n < (u32)nnode) ? (((u32)(bin * NB) + n) * 2048u + (dg == 0 ? 1u : 0u)) : 0xFFFFFFFFu;
    }
    const char* xb = (const char*)x + sl * 128;
    const char* rb = (const char*)(rec + (size_t)hb * RSTR * 4);
    char* pb = (char*)part + (size_t)h * NN * 2048 + sl * 128 + li * 16;
    const u32 loff = li * 16;
    const u32x4* slot4 = (const u32x4*)L.slot;
    u32x4* stg = &L.stage[wv][0][0];
    const int aoff = g * 32 + (lane & 3);
    const int eoff = ((2 * ((lane & 15) >> 2) + (lane >> 5)) * 4 + (lane & 3)) * 4 + 2 + ((lane >> 4) & 1);

    u32x4 X[4][4];
    u32x4 P0, P1;
    f32x4 acc[8];
#pragma unroll
    for (int j = 0; j < 8; j++) acc[j] = f32x4{0.f, 0.f, 0.f, 0.f};

    auto issue_x = [&](int r, u32 ent) {
      const u32 off = (ent & 0x3FFFu) * 2048u + loff;
#pragma unroll
      for (int u = 0; u < 4; u++) X[r][u] = *(const u32x4*)(xb + off + u * 512);
    };
    auto issue_p = [&](u32 quad) {
      const u32 e0 = L.slot[quad * 32 + (lane >> 2)], e1 = L.slot[quad * 32 + 16 + (lane >> 2)];
      P0 = *(const u32x4*)(rb + (e0 >> 14) * 64u + (lane & 3) * 16u);
      P1 = *(const u32x4*)(rb + (e1 >> 14) * 64u + (lane & 3) * 16u);
    };
    issue_p(q0);
    {
      const u32x4 en = slot4[q0 * 8 + g];
#pragma unroll
      for (int r = 0; r < 4; r++) issue_x(r, en[r]);
    }
    stg[lane] = P0; stg[64 + lane] = P1;
    issue_p(q0 + (nq > 1 ? 1 : 0));

    u32 it = 0;
    auto quad = [&](int Q, auto LOADT) {
      constexpr bool LOAD = decltype(LOADT)::value;
      const int cb = (Q & 1) * 128;
      u32x4 en;
      if constexpr (LOAD) {
        en = slot4[(q0 + Q + 1) * 8 + g];
        stg[(cb ^ 128) + lane] = P0; stg[(cb ^ 128) + 64 + lane] = P1;
        issue_p(q0 + (Q + 2 < nq ? Q + 2 : nq - 1));
      }
      const u32x4 eav = stg[cb + eoff];
      f32x4 rw[8];
#pragma unroll
      for (int j = 0; j < 8; j++)
        rw[j] = __builtin_amdgcn_mfma_f32_16x16x32_bf16(__builtin_bit_cast(bf16x8, eav), __builtin_bit_cast(bf16x8, Bj[j]),
                                                        f32x4{0.f, 0.f, 0.f, 0.f}, 0, 0, 0);
#pragma unroll
      for (int r = 0; r < 4; r++) {
        const u32x2 av = ((const u32x2*)(stg + cb))[aoff + r * 8];
        u32x2 Bp[8];
#pragma unroll
        for (int p = 0; p < 4; p++) {
          Bp[2 * p].x = __builtin_amdgcn_perm(X[r][1][p], X[r][0][p], 0x05040100u);
          Bp[2 * p].y = __builtin_amdgcn_perm(X[r][3][p], X[r][2][p], 0x05040100u);
          Bp[2 * p + 1].x = __builtin_amdgcn_perm(X[r][1][p], X[r][0][p], 0x07060302u);
          Bp[2 * p + 1].y = __builtin_amdgcn_perm(X[r][3][p], X[r][2][p], 0x07060302u);
        }
        if constexpr (LOAD) issue_x(r, en[r]);
#pragma unroll
        for (int j = 0; j < 8; j++) {
          const f32x4 D = __builtin_amdgcn_mfma_f32_4x4x4bf16_1k(__builtin_bit_cast(s16x4, av), __builtin_bit_cast(s16x4, Bp[j]),
                                                                 f32x4{0.f, 0.f, 0.f, 0.f}, 0, 0, 0);
          acc[j] += D * rw[j][r];
        }
        it++;
        if (it == te[0]) {
          const u32 n = nd[0];
          if (n != 0xFFFFFFFFu) {
            const bool z = (n & 1u) != 0;
#pragma unroll
            for (int w = 0; w < 4; w++) {
              u32x4 o;
              o.x = pack2(acc[0][w], acc[1][w]); o.y = pack2(acc[2][w], acc[3][w]);
              o.z = pack2(acc[4][w], acc[5][w]); o.w = pack2(acc[6][w], acc[7][w]);
              if (z) o = u32x4{0, 0, 0, 0};
              *(u32x4*)(pb + (n & ~1u) + w * 512) = o;
            }
          }
#pragma unroll
          for (int j = 0; j < 8; j++) acc[j] = f32x4{0.f, 0.f, 0.f, 0.f};
          te[0] = te[1]; te[1] = te[2]; te[2] = te[3]; te[3] = te[4]; te[4] = 0xFFFFFFFFu;
          nd[0] = nd[1]; nd[1] = nd[2]; nd[2] = nd[3]; nd[3] = nd[4];
        }
      }
    };
    for (int Q = 0; Q < nq - 1; Q++) quad(Q, std::true_type{});
    quad(nq - 1, std::false_type{});
    return;
  }

  {
    float* accs = (float*)&L.stage[0][0][0];
    u32* list = L.slot;
    const int w = (t >> 6) & 3, cch = sl * 64 + (t & 63);
    const u16* xs = (const u16*)x;
    const u16* wrs = (const u16*)wr;
    for (int n0 = 0; n0 < nnode; n0 += 32) {
      const int lo = bin * NB + n0;
      const int hi = min(lo + 32, bin * NB + nnode);
      for (int i = t; i < 32 * 256; i += 512) accs[i] = 0.f;
      for (int e0 = 0; e0 < NE; e0 += 512) {
        const int e = e0 + t;
        bool mt = false;
        if (e < NE) {
          const int d = dst[e], s = src[e];
          mt = (d >= lo) && (d < hi) && ((s >= HALF ? 1 : 0) == h);
        }
        const unsigned long long bal = __ballot(mt);
        if (lane == 0) L.wcnt[wv] = (u32)__popcll(bal);
        __syncthreads();
        u32 off = 0, tot = 0;
        for (int i = 0; i < 8; i++) { const u32 cn = L.wcnt[i]; if (i < wv) off += cn; tot += cn; }
        if (mt) list[off + (u32)__popcll(bal & ((1ull << lane) - 1ull))] = (u32)e;
        __syncthreads();
        if (t < 256) {
          for (u32 i = 0; i < tot; i++) {
            const int ee = (int)list[i];
            const int s = src[ee], d = dst[ee];
            float rwv = 0.f;
            for (int k = 0; k < 16; k++) rwv += bf1(ea[(size_t)ee * 16 + k]) * bf1(wrs[k * 256 + cch]);
            float tp = 0.f;
            for (int u = 0; u < 4; u++) {
              float a = 0.f;
              for (int v = 0; v < 4; v++) a += bf1(cg[(u * 4 + v) * 4 + w]) * bf1(sh[(size_t)ee * 4 + v]);
              tp += a * bf1(xs[((size_t)s * 4 + u) * 256 + cch]);
            }
            accs[(d - lo) * 256 + t] += tp * rwv;
          }
        }
        __syncthreads();
      }
      if (t < 256) {
        u16* ps = (u16*)part;
        for (int n = lo; n < hi; n++)
          ps[(((size_t)h * NN + n) * 4 + w) * 256 + cch] = (u16)(pack2(accs[(n - lo) * 256 + t], 0.f) & 0xFFFFu);
      }
      __syncthreads();
    }
  }
}

__device__ __forceinline__ u32 addbf2(u32 a, u32 b) { return pack2(bflo(a) + bflo(b), bfhi(a) + bfhi(b)); }
__device__ __forceinline__ u32x4 addbf8(u32x4 a, u32x4 b) {
  u32x4 r;
  r.x = addbf2(a.x, b.x); r.y = addbf2(a.y, b.y); r.z = addbf2(a.z, b.z); r.w = addbf2(a.w, b.w);
  return r;
}

__global__ __launch_bounds__(512, 2) void k3(const u32x4* __restrict__ part, const u32x4* __restrict__ wfrag,
                                             u32x4* __restrict__ out) {
  __shared__ u32x4 tl[2][32 * 32];
  const int t = threadIdx.x, b = blockIdx.x;
  const int lane = t & 63, wv = t >> 6;
  const int row = t >> 4, c16 = t & 15;
  const u32x4* p0 = part + row * 32 + c16;
  const u32x4* p1 = p0 + (size_t)NN * 128;
  u32x4 R[2][4];
  auto ld = [&](int s, int tile) {
    const size_t o = (size_t)tile * 1024;
    R[s][0] = p0[o]; R[s][1] = p0[o + 16]; R[s][2] = p1[o]; R[s][3] = p1[o + 16];
  };
  ld(0, b);
  ld(1, b + 256);
  u32x4 W[2][8];
#pragma unroll
  for (int p = 0; p < 2; p++)
#pragma unroll
    for (int ks = 0; ks < 8; ks++) W[p][ks] = wfrag[((wv * 2 + p) * 8 + ks) * 64 + lane];
  const bool five = b < (1250 - 1024);
  const int wo = row * 32 + (c16 ^ (row & 15));
  const int ro = (lane & 15) * 32;
  auto step = [&](int i, int pre) {
    const int s = i & 1;
    tl[s][wo] = addbf8(R[s][0], R[s][2]);
    tl[s][wo + 16] = addbf8(R[s][1], R[s][3]);
    if (pre >= 0) ld(s, pre);
    __syncthreads();
    f32x4 acc[2][2];
#pragma unroll
    for (int rt = 0; rt < 2; rt++)
#pragma unroll
      for (int p = 0; p < 2; p++) acc[rt][p] = f32x4{0.f, 0.f, 0.f, 0.f};
#pragma unroll
    for (int ks = 0; ks < 8; ks++) {
      const int pc = ks * 4 + (lane >> 4);
      const int sw = (pc & 16) | ((pc ^ lane) & 15);
#pragma unroll
      for (int rt = 0; rt < 2; rt++) {
        const u32x4 bv = tl[s][rt * 512 + ro + sw];
#pragma unroll
        for (int p = 0; p < 2; p++)
          acc[rt][p] = __builtin_amdgcn_mfma_f32_16x16x32_bf16(__builtin_bit_cast(bf16x8, W[p][ks]),
                                                               __builtin_bit_cast(bf16x8, bv), acc[rt][p], 0, 0, 0);
      }
    }
    const int tile = b + 256 * i;
#pragma unroll
    for (int rt = 0; rt < 2; rt++) {
      u32x4 o;
      o.x = pack2(acc[rt][0][0], acc[rt][0][1]); o.y = pack2(acc[rt][0][2], acc[rt][0][3]);
      o.z = pack2(acc[rt][1][0], acc[rt][1][1]); o.w = pack2(acc[rt][1][2], acc[rt][1][3]);
      out[((size_t)tile * 32 + rt * 16 + (lane & 15)) * 32 + wv * 4 + (lane >> 4)] = o;
    }
  };
  step(0, b + 512);
  step(1, b + 768);
  step(2, five ? b + 1024 : b);
  step(3, -1);
  if (five) step(4, -1);
}

extern "C" void kernel_launch(void* const* d_in, const int* in_sizes, int n_in, void* d_out, int out_size,
                              void* d_ws, size_t ws_size, hipStream_t stream) {
  const u32x4* x = (const u32x4*)d_in[0];
  const int* src = (const int*)d_in[1];
  const int* dst = (const int*)d_in[2];
  char* ws = (char*)d_ws;
  u32* meta = (u32*)(ws + WS_META);
  u32x4* wfrag = (u32x4*)(ws + WS_WFRAG);
  u32x4* rec = (u32x4*)(ws + WS_REC);
  u32x4* part = (u32x4*)(ws + WS_PART);
  u32* sink = (u32*)(ws + WS_SINK);
  k1<<<NCH, 640, 0, stream>>>(x, src, dst, (const u32x2*)d_in[3], (const u32x4*)d_in[4], (const u32*)d_in[5],
                              (const u16*)d_in[7], meta, rec, wfrag, sink);
  k2<<<256, 512, 0, stream>>>(x, meta, rec, (const u32x4*)d_in[6], part, src, dst, (const u16*)d_in[3],
                              (const u16*)d_in[4], (const u16*)d_in[5]);
  k3<<<256, 512, 0, stream>>>(part, wfrag, (u32x4*)d_out);
}
```

```cpp
#include <hip/hip_runtime.h>
#include <stdint.h>

typedef unsigned int u32;
typedef unsigned short u16;
typedef unsigned char u8;
typedef u32 __attribute__((ext_vector_type(4))) u32x4;
typedef u32 __attribute__((ext_vector_type(2))) u32x2;
typedef float __attribute__((ext_vector_type(4))) f32x4;
typedef float __attribute__((ext_vector_type(2))) f32x2;
typedef short __attribute__((ext_vector_type(4))) s16x4;
typedef __bf16 __attribute__((ext_vector_type(8))) bf16x8;
typedef __bf16 __attribute__((ext_vector_type(2))) bf16x2;

#define AUX_K1 16
#define AUX_K2 16
#define AUX_K3 0
#define PFG 64
#define PFS 0
#define PFT (128 / PFG)
#define PF1 ((PFS * PFT + 639) / 640)
#define PF2 (((625 - PFS) * PFT + 511) / 512)

constexpr int NN = 10000, NE = 160000, HALF = 5000;
constexpr int NB = 313;
constexpr int NCH = 256, CH = 625;
constexpr int CAPC = 32;
constexpr int CELLS = NCH * CAPC;
constexpr int RSTR = CELLS + 2;
constexpr u32 SENT = 0x003FFFFFu;
constexpr u32 OVF = 0x003FDFFFu;
constexpr int ITCAP = 512;

constexpr size_t WS_META = 0;
constexpr size_t WS_WFRAG = WS_META + (size_t)64 * CELLS * 4;
constexpr size_t WS_REC = WS_WFRAG + 131072;
constexpr size_t REC_BYTES = (size_t)64 * RSTR * 64;
constexpr size_t WS_PART = WS_REC + REC_BYTES;
constexpr size_t PART_BYTES = (size_t)2 * NN * 2048;
constexpr size_t WS_SINK = WS_PART + PART_BYTES;

__device__ __forceinline__ u32 pack2(float a, float b) {
  f32x2 v = {a, b};
  bf16x2 r = __builtin_convertvector(v, bf16x2);
  return __builtin_bit_cast(u32, r);
}
__device__ __forceinline__ float bflo(u32 v) { return __uint_as_float(v << 16); }
__device__ __forceinline__ float bfhi(u32 v) { return __uint_as_float(v & 0xFFFF0000u); }
__device__ __forceinline__ float bf1(u16 v) { return __uint_as_float((u32)v << 16); }

template <int CTRL, int RM, bool BC>
__device__ __forceinline__ u32 dpp0(u32 v) { return (u32)__builtin_amdgcn_update_dpp(0, (int)v, CTRL, RM, 0xf, BC); }
__device__ __forceinline__ u32 wave_incl_scan(u32 v) {
  v += dpp0<0x111, 0xf, true>(v);
  v += dpp0<0x112, 0xf, true>(v);
  v += dpp0<0x114, 0xf, true>(v);
  v += dpp0<0x118, 0xf, true>(v);
  v += dpp0<0x142, 0xa, false>(v);
  v += dpp0<0x143, 0xc, false>(v);
  return v;
}
__device__ __forceinline__ u32 mbcnt64(u32 lo, u32 hi) {
  return __builtin_amdgcn_mbcnt_hi(hi, __builtin_amdgcn_mbcnt_lo(lo, 0u));
}

#define TS(i) do { } while (0)
#define DBGARG

struct K1Lds {
  u32x4 rs[CH * 4 + 4];
  u32x4 ms[64 * 8];
  u32x4 cw[64];
  u32 dest[CH + 3];
};

__global__ __launch_bounds__(640) void k1(const u32x4* __restrict__ x, const int* __restrict__ src,
                                          const int* __restrict__ dst, const u32x2* __restrict__ sh,
                                          const u32x4* __restrict__ ea, const u32* __restrict__ cg,
                                          const u16* __restrict__ wout, u32* __restrict__ meta,
                                          u32x4* __restrict__ rec, u32x4* __restrict__ wfrag,
                                          u32* __restrict__ sink DBGARG) {
  __shared__ K1Lds L;
  const int t = threadIdx.x, c = blockIdx.x;
  const int lane = t & 63, wv = __builtin_amdgcn_readfirstlane(t >> 6);
  TS(8);
  const bool act = t < CH;
  const int e = c * CH + (act ? t : 0);
  const int d = dst[e], s = src[e];
  const u32x2 shv = sh[e];
  const u32x4 ea0 = ea[2 * e], ea1 = ea[2 * e + 1];
  if (t < 512) L.ms[t] = u32x4{SENT, SENT, SENT, SENT};
  else if (t < 576) L.cw[t - 512] = u32x4{0, 0, 0, 0};
  if (t >= 608) {
    const int item = c * 32 + (t - 608);
    const int tile = item >> 9, ks = (item >> 6) & 7, l = item & 63;
    const int rho = l & 15;
    const int dd = 32 * (tile >> 1) + 8 * (rho >> 2) + 4 * (tile & 1) + (rho & 3);
    const u16* wp = wout + (size_t)(32 * ks + 8 * (l >> 4)) * 256 + dd;
    u32x4 v;
    v.x = (u32)wp[0] | ((u32)wp[256] << 16);
    v.y = (u32)wp[512] | ((u32)wp[768] << 16);
    v.z = (u32)wp[1024] | ((u32)wp[1280] << 16);
    v.w = (u32)wp[1536] | ((u32)wp[1792] << 16);
    wfrag[item] = v;
  }
  if (c < 64 && t >= 576 && t < 580) rec[((size_t)c * RSTR + CELLS) * 4 + (t - 576)] = u32x4{0, 0, 0, 0};
  __syncthreads();
  const int bin = d / NB, ld = d - bin * NB;
  const u32 hf = (s >= HALF ? 1u : 0u);
  const u32 hb = (u32)bin * 2 + hf;
  u32 r;
  {
    const unsigned long long am = __ballot(act);
    u32 lo = (u32)am, hi = (u32)(am >> 32);
#pragma unroll
    for (int bb = 0; bb < 6; bb++) {
      const bool bit = (hb >> bb) & 1u;
      const unsigned long long bal = __ballot(bit);
      const u32 bm = bit ? 0xFFFFFFFFu : 0u;
      lo &= ~((u32)bal ^ bm);
      hi &= ~((u32)(bal >> 32) ^ bm);
    }
    r = mbcnt64(lo, hi);
    if (act && r == 0) ((u8*)L.cw)[hb * 16 + wv] = (u8)(__popc(lo) + __popc(hi));
  }
  u32x4 a0, a1;
  {
    const float s0 = bflo(shv.x), s1 = bfhi(shv.x), s2 = bflo(shv.y), s3 = bfhi(shv.y);
    float A[4][4];
#pragma unroll
    for (int u = 0; u < 4; u++) {
#pragma unroll
      for (int wp = 0; wp < 2; wp++) {
        const u32 c0 = cg[u * 8 + 0 + wp], c1 = cg[u * 8 + 2 + wp], c2 = cg[u * 8 + 4 + wp], c3 = cg[u * 8 + 6 + wp];
        A[2 * wp][u] = bflo(c0) * s0 + bflo(c1) * s1 + bflo(c2) * s2 + bflo(c3) * s3;
        A[2 * wp + 1][u] = bfhi(c0) * s0 + bfhi(c1) * s1 + bfhi(c2) * s2 + bfhi(c3) * s3;
      }
    }
    a0.x = pack2(A[0][0], A[0][1]); a0.y = pack2(A[0][2], A[0][3]);
    a0.z = pack2(A[1][0], A[1][1]); a0.w = pack2(A[1][2], A[1][3]);
    a1.x = pack2(A[2][0], A[2][1]); a1.y = pack2(A[2][2], A[2][3]);
    a1.z = pack2(A[3][0], A[3][1]); a1.w = pack2(A[3][2], A[3][3]);
  }
  __syncthreads();
  TS(9);
  {
    const u32x4 cv = L.cw[lane];
    const u32 m0 = wv >= 4 ? 0x01010101u : (0x01010101u & ((1u << (8 * wv)) - 1u));
    const u32 m1 = wv >= 8 ? 0x01010101u : (wv <= 4 ? 0u : (0x01010101u & ((1u << (8 * (wv - 4))) - 1u)));
    const u32 m2 = wv <= 8 ? 0u : 0x00000001u;
    const u32 tot = __builtin_amdgcn_udot4(cv.x, 0x01010101u, __builtin_amdgcn_udot4(cv.y, 0x01010101u,
                    __builtin_amdgcn_udot4(cv.z, 0x01010101u, 0u, false), false), false);
    const u32 pre = __builtin_amdgcn_udot4(cv.x, m0, __builtin_amdgcn_udot4(cv.y, m1,
                    __builtin_amdgcn_udot4(cv.z, m2, 0u, false), false), false);
    const u32 inc = wave_incl_scan(tot);
    const u32 pk = (inc - tot + pre) | (pre << 10) | (tot > CAPC ? (1u << 20) : 0u);
    if (wv == 0 && tot > CAPC) {
#pragma unroll
      for (int i = 0; i < 8; i++) L.ms[lane * 8 + i] = u32x4{OVF, OVF, OVF, OVF};
    }
    const u32 v = (u32)__builtin_amdgcn_ds_bpermute((int)(hb * 4), (int)pk);
    if (act) {
      const u32 p = (v & 1023u) + r, rank = ((v >> 10) & 1023u) + r;
      const u32 sw = (p >> 1) & 3u;
      L.rs[p * 4 + (0 ^ sw)] = a0; L.rs[p * 4 + (1 ^ sw)] = a1;
      L.rs[p * 4 + (2 ^ sw)] = ea0; L.rs[p * 4 + (3 ^ sw)] = ea1;
      const bool ok = (v >> 20) == 0;
      L.dest[p] = ok ? (hb * RSTR + (u32)c * CAPC + rank) * 64u : 0xF0000000u;
      if (ok) ((u32*)L.ms)[hb * CAPC + rank] = (u32)(s - (int)hf * HALF) | ((u32)ld << 13);
    }
  }
  __syncthreads();
  TS(10);
  {
    const __amdgpu_buffer_rsrc_t rr = __builtin_amdgcn_make_buffer_rsrc((void*)rec, 0, (int)REC_BYTES, 0x00020000);
    const __amdgpu_buffer_rsrc_t rm = __builtin_amdgcn_make_buffer_rsrc((void*)meta, 0, 64 * CELLS * 4, 0x00020000);
    if (t < 512)
      __builtin_amdgcn_raw_buffer_store_b128(L.ms[t], rm, ((u32)(t >> 3) * CELLS + (u32)c * CAPC) * 4u + (t & 7) * 16u, 0, AUX_K1);
#pragma unroll
    for (int k = 0; k < 4; k++) {
      const int i = t + 640 * k;
      if (i < CH * 4) {
        const int p = i >> 2, j = i & 3;
        const u32 dd = L.dest[p];
        const u32x4 v = L.rs[p * 4 + (j ^ ((p >> 1) & 3))];
        __builtin_amdgcn_raw_buffer_store_b128(v, rr, dd | (u32)(j * 16), 0, AUX_K1);
      }
    }
  }
  TS(11);
}

struct K2Lds {
  u32 slot[ITCAP * 8];
  u32x4 stage[8][2][128];
  u32x4 cnt[320];
  u32 anysrc[320];
  u32 dh[64];
  u32 wt[8][2][64];
  u16 sorted[320];
  u32 flag;
  u32 wcnt[8];
};

__device__ __forceinline__ u32 slot_index(u32 it, u32 g) { return ((it >> 2) << 5) + (g << 2) + (it & 3); }

constexpr int NG = 8;

__global__ __launch_bounds__(512, 2) void k2(const u32x4* __restrict__ x, const u32* __restrict__ meta,
                                             const u32x4* __restrict__ rec, const u32x4* __restrict__ wr,
                                             u32x4* __restrict__ part, const int* __restrict__ src,
                                             const int* __restrict__ dst, const u16* __restrict__ sh,
                                             const u16* __restrict__ ea, const u16* __restrict__ cg,
                                             u32* __restrict__ sink DBGARG) {
  __shared__ K2Lds L;
  const int t = threadIdx.x, b = blockIdx.x;
  const int lane = t & 63, wv = __builtin_amdgcn_readfirstlane(t >> 6);
  const int sl = (b & 7) >> 1, h = b & 1, bin = b >> 3;
  const int hb = bin * 2 + h;
  const int nnode = (bin == 31) ? (NN - 31 * NB) : NB;
  TS(0);
  u32x4 m[4];
  {
    const u32x4* mp = (const u32x4*)(meta + (size_t)hb * CELLS);
#pragma unroll
    for (int k = 0; k < 4; k++) m[k] = mp[t + 512 * k];
  }
  __builtin_amdgcn_sched_barrier(0);
  u32x4 wrow[8];
  {
    const int q = lane >> 4, col = lane & 15;
    const u32x4* wp = wr + (size_t)(8 * (q & 1)) * 32 + sl * 8 + (col & 7);
#pragma unroll
    for (int jj = 0; jj < 8; jj++) wrow[jj] = wp[jj * 32];
  }
  const char* xb = (const char*)x + (size_t)h * HALF * 2048 + sl * 128;
  u32 pf[PF2];
  {
#pragma unroll
    for (int k = 0; k < PF2; k++) {
      int i = PFS * PFT + t + 512 * k;
      i = i < 625 * PFT ? i : 625 * PFT - 1;
      pf[k] = *(const u32*)(xb + (u32)(bin * 625 + i / PFT) * 512u + (i % PFT) * PFG);
    }
  }
  __builtin_amdgcn_sched_barrier(0);
  if (t < 320) L.cnt[t] = u32x4{0, 0, 0, 0};
  else if (t < 384) L.dh[t - 320] = 0;
  else if (t == 384) L.flag = 0;
  __syncthreads();
  u32x4* list = &L.stage[wv][0][0];
  u32 np = 0;
#pragma unroll
  for (int k = 0; k < 4; k++) {
    const u32 pid = (u32)t + 512u * k;
    const bool ne = m[k].x != SENT;
    const unsigned long long bal = __ballot(ne);
    u32x4 v = m[k];
    v.x |= (pid & 1023u) << 22;
    v.y |= (pid >> 10) << 22;
    if (ne) list[np + mbcnt64((u32)bal, (u32)(bal >> 32))] = v;
    np += (u32)__popcll(bal);
  }
  TS(1);
  const u32 ns = np * 4;
  u32 ev[NG], el[NG], rk[NG];
  const u32 csh = (wv & 1) * 16;
  {
    bool bad = ns > NG * 64;
#pragma unroll
    for (int k = 0; k < NG; k++) {
      el[k] = 511; ev[k] = 0; rk[k] = 0;
      if (k * 64 < ns) {
        const u32 i = k * 64 + lane;
        const u32 v = ((const u32*)list)[i];
        const u32 pid = (dpp0<0x00, 0xf, false>(v) >> 22) | ((dpp0<0x55, 0xf, false>(v) >> 22) << 10);
        const u32 ld = (i < ns) ? ((v >> 13) & 511u) : 511u;
        bad |= (ld == 510);
        el[k] = ld;
        ev[k] = (v & 0x1FFFu) | ((pid * 4 + (lane & 3)) << 13);
        if (ld < 320) {
          rk[k] = (atomicAdd(&((u32*)L.cnt)[ld * 4 + (wv >> 1)], 1u << csh) >> csh) & 0xFFFFu;
          L.anysrc[ld] = v & 0x1FFFu;
        }
      }
    }
    if (bad) L.flag = 1;
  }
  __syncthreads();
  TS(2);
  u32 mydeg = 0, r2 = 0, pre[8];
  if (t < 320) {
    const u32x4 cv = L.cnt[t];
#pragma unroll
    for (int w = 0; w < 8; w++) {
      pre[w] = mydeg;
      mydeg += (cv[w >> 1] >> ((w & 1) * 16)) & 0xFFFFu;
    }
    if (mydeg > 63) { L.flag = 1; mydeg = 63; }
    r2 = atomicAdd(&L.dh[63 - mydeg], 1u);
  }
  __syncthreads();
  TS(3);
  u32 hst, binc, blen_l;
  bool slow;
  {
    const u32 hv = L.dh[lane];
    const u32 hinc = wave_incl_scan(hv);
    hst = hinc - hv;
    for (u32 bt = (hst + 7) >> 3; bt < ((hinc + 7) >> 3); bt++) L.wt[wv][0][bt] = (lane == 63) ? 1u : (u32)(63 - lane);
    const int w = lane >> 3, k = lane & 7;
    const int bt = (8 * k + ((k & 1) ? 7 - w : w)) % 40;
    u32 len = L.wt[wv][0][bt];
    if (k >= 5) len = 0;
    u32 tot = len;
    tot += dpp0<0xB1, 0xf, false>(tot);
    tot += dpp0<0x4E, 0xf, false>(tot);
    tot += dpp0<0x141, 0xf, false>(tot);
    if (k == 5) len = (4 - (tot & 3)) & 3;
    binc = wave_incl_scan(len);
    blen_l = len;
    if (k < 5) L.wt[wv][1][bt] = (binc - len) | (len << 16);
    slow = (L.flag != 0) || ((u32)__builtin_amdgcn_readlane((int)binc, 63) > ITCAP);
  }
#pragma unroll
  for (int k = 0; k < PF2; k++) asm volatile("" ::"v"(pf[k]));
  if (!slow) {
    const u32 mypos = (u32)__builtin_amdgcn_ds_bpermute((int)((63 - mydeg) * 4), (int)hst) + r2;
    if (t < 320) {
      const u32 bl = L.wt[wv][1][mypos >> 3];
      const u32 bs = bl & 0xFFFFu, len = bl >> 16, g = mypos & 7;
      const u32 o = bs | (g << 10);
      u32x4 pw;
#pragma unroll
      for (int i = 0; i < 4; i++) pw[i] = (o + pre[2 * i]) | ((o + pre[2 * i + 1]) << 16);
      L.cnt[t] = pw;
      L.sorted[mypos] = (u16)(t | (mydeg == 0 ? 0x8000 : 0));
      if (mydeg < len) {
        const u32 ps = (mydeg > 0 ? L.anysrc[t] : 0u) | ((u32)CELLS << 13);
        for (u32 r = mydeg; r < len; r++) L.slot[slot_index(bs + r, g)] = ps;
      }
    }
    if (lane == wv * 8 + 5) {
      for (u32 i = 0; i < blen_l; i++)
#pragma unroll
        for (int g = 0; g < 8; g++) L.slot[slot_index(binc - blen_l + i, g)] = (u32)CELLS << 13;
    }
    __syncthreads();
#pragma unroll
    for (int k = 0; k < NG; k++) {
      if (k * 64 < ns && el[k] < 320) {
        const u32 pw = ((const u16*)L.cnt)[el[k] * 8 + wv];
        L.slot[slot_index((pw & 1023u) + rk[k], pw >> 10)] = ev[k];
      }
    }
    const int g = lane >> 3, li = lane & 7;
    u32x4 Bj[8];
    {
      const bool nz = ((lane >> 5) == ((lane >> 3) & 1));
#pragma unroll
      for (int j = 0; j < 8; j++) {
#pragma unroll
        for (int p = 0; p < 4; p++) {
          const u32 lo = wrow[2 * p][j >> 1], hi = wrow[2 * p + 1][j >> 1];
          const u32 v = (j & 1) ? __builtin_amdgcn_perm(hi, lo, 0x07060302u) : __builtin_amdgcn_perm(hi, lo, 0x05040100u);
          Bj[j][p] = nz ? v : 0u;
        }
      }
    }
    const u32 it0 = (u32)__builtin_amdgcn_readlane((int)(binc - blen_l), wv * 8);
    const u32 q0 = it0 >> 2;
    const int nq = (int)(((u32)__builtin_amdgcn_readlane((int)binc, wv * 8 + 5) - it0) >> 2);
    u32 te[5];
#pragma unroll
    for (int k = 0; k < 5; k++) te[k] = (u32)__builtin_amdgcn_readlane((int)binc, wv * 8 + k) - it0;
    __syncthreads();
    TS(4);
    u32 nd[5];
#pragma unroll
    for (int k = 0; k < 5; k++) {
      const int bt = 8 * k + ((k & 1) ? 7 - wv : wv);
      const u32 sv = L.sorted[bt * 8 + g];
      const u32 n = sv & 0x7FFFu;
      nd[k] = (n < (u32)nnode) ? (((u32)(h * NN + bin * NB) + n) * 2048u + sl * 128 + li * 16 + (sv >> 15)) : 0xF0000000u;
    }
    const char* rb = (const char*)(rec + (size_t)hb * RSTR * 4);
    const __amdgpu_buffer_rsrc_t rp = __builtin_amdgcn_make_buffer_rsrc((void*)part, 0, (int)PART_BYTES, 0x00020000);
    const u32 loff = li * 16;
    const u32x4* slot4 = (const u32x4*)L.slot;
    u32x4* stg = &L.stage[wv][0][0];
    const int aoff = g * 32 + (lane & 3);
    const int eoff = ((2 * ((lane & 15) >> 2) + (lane >> 5)) * 4 + (lane & 3)) * 4 + 2 + ((lane >> 4) & 1);

    u32x4 X[4][4];
    u32x4 P0, P1;
    f32x4 acc[8];
#pragma unroll
    for (int j = 0; j < 8; j++) acc[j] = f32x4{0.f, 0.f, 0.f, 0.f};

    auto issue_x = [&](int r, u32 ent) {
      const u32 off = (ent & 0x1FFFu) * 2048u + loff;
#pragma unroll
      for (int u = 0; u < 4; u++) X[r][u] = *(const u32x4*)(xb + off + u * 512);
    };
    auto issue_p = [&](u32 quad) {
      const u32 e0 = L.slot[quad * 32 + (lane >> 2)], e1 = L.slot[quad * 32 + 16 + (lane >> 2)];
      P0 = *(const u32x4*)(rb + (e0 >> 13) * 64u + (lane & 3) * 16u);
      P1 = *(const u32x4*)(rb + (e1 >> 13) * 64u + (lane & 3) * 16u);
    };
    issue_p(q0);
    const u32x4 Pa = P0, Pb = P1;
    __builtin_amdgcn_sched_barrier(0);
    issue_p(q0 + (nq > 1 ? 1 : 0));
    __builtin_amdgcn_sched_barrier(0);
    {
      const u32x4 en = slot4[q0 * 8 + g];
#pragma unroll
      for (int r = 0; r < 4; r++) { issue_x(r, en[r]); __builtin_amdgcn_sched_barrier(0); }
    }
    stg[lane] = Pa; stg[64 + lane] = Pb;
    asm volatile("" : "+v"(P0), "+v"(P1));
    __builtin_amdgcn_sched_barrier(0);

    u32 it = 0;
    auto quad = [&](int Q, auto LOADT) {
      constexpr bool LOAD = decltype(LOADT)::value;
      const int cb = (Q & 1) * 128;
      u32x4 en;
      if constexpr (LOAD) {
        en = slot4[(q0 + Q + 1) * 8 + g];
        stg[(cb ^ 128) + lane] = P0; stg[(cb ^ 128) + 64 + lane] = P1;
        issue_p(q0 + (Q + 2 < nq ? Q + 2 : nq - 1));
        __builtin_amdgcn_sched_barrier(0);
      }
      const u32x4 eav = stg[cb + eoff];
      f32x4 rw[8];
#pragma unroll
      for (int j = 0; j < 8; j++)
        rw[j] = __builtin_amdgcn_mfma_f32_16x16x32_bf16(__builtin_bit_cast(bf16x8, eav), __builtin_bit_cast(bf16x8, Bj[j]),
                                                        f32x4{0.f, 0.f, 0.f, 0.f}, 0, 0, 0);
#pragma unroll
      for (int r = 0; r < 4; r++) {
        const u32x2 av = ((const u32x2*)(stg + cb))[aoff + r * 8];
        u32x2 Bp[8];
#pragma unroll
        for (int p = 0; p < 4; p++) {
          Bp[2 * p].x = __builtin_amdgcn_perm(X[r][1][p], X[r][0][p], 0x05040100u);
          Bp[2 * p].y = __builtin_amdgcn_perm(X[r][3][p], X[r][2][p], 0x05040100u);
          Bp[2 * p + 1].x = __builtin_amdgcn_perm(X[r][1][p], X[r][0][p], 0x07060302u);
          Bp[2 * p + 1].y = __builtin_amdgcn_perm(X[r][3][p], X[r][2][p], 0x07060302u);
        }
        if constexpr (LOAD) issue_x(r, en[r]);
        f32x4 D[8];
#pragma unroll
        for (int j = 0; j < 8; j++)
          D[j] = __builtin_amdgcn_mfma_f32_4x4x4bf16_1k(__builtin_bit_cast(s16x4, av), __builtin_bit_cast(s16x4, Bp[j]),
                                                        f32x4{0.f, 0.f, 0.f, 0.f}, 0, 0, 0);
        __builtin_amdgcn_sched_barrier(0);
#pragma unroll
        for (int j = 0; j < 8; j++) acc[j] += D[j] * rw[j][r];
        it++;
        if (it == te[0]) {
          const u32 n = nd[0];
          const bool z = (n & 1u) != 0;
#pragma unroll
          for (int w = 0; w < 4; w++) {
            u32x4 o;
            o.x = pack2(acc[0][w], acc[1][w]); o.y = pack2(acc[2][w], acc[3][w]);
            o.z = pack2(acc[4][w], acc[5][w]); o.w = pack2(acc[6][w], acc[7][w]);
            if (z) o = u32x4{0, 0, 0, 0};
            __builtin_amdgcn_raw_buffer_store_b128(o, rp, (n & ~1u) + w * 512, 0, AUX_K2);
          }
#pragma unroll
          for (int j = 0; j < 8; j++) acc[j] = f32x4{0.f, 0.f, 0.f, 0.f};
          te[0] = te[1]; te[1] = te[2]; te[2] = te[3]; te[3] = te[4]; te[4] = 0xFFFFFFFFu;
          nd[0] = nd[1]; nd[1] = nd[2]; nd[2] = nd[3]; nd[3] = nd[4];
        }
      }
    };
    for (int Q = 0; Q < nq - 1; Q++) quad(Q, std::true_type{});
    quad(nq - 1, std::false_type{});
    return;
  }

  {
    __syncthreads();
    float* accs = (float*)&L.stage[0][0][0];
    u32* elist = L.slot;
    const int w = (t >> 6) & 3, cch = sl * 64 + (t & 63);
    const u16* xs = (const u16*)x;
    const u16* wrs = (const u16*)wr;
    for (int n0 = 0; n0 < nnode; n0 += 32) {
      const int lo = bin * NB + n0;
      const int hi = min(lo + 32, bin * NB + nnode);
      for (int i = t; i < 32 * 256; i += 512) accs[i] = 0.f;
      for (int e0 = 0; e0 < NE; e0 += 512) {
        const int e = e0 + t;
        bool mt = false;
        if (e < NE) {
          const int d = dst[e], s = src[e];
          mt = (d >= lo) && (d < hi) && ((s >= HALF ? 1 : 0) == h);
        }
        const unsigned long long bal = __ballot(mt);
        if (lane == 0) L.wcnt[wv] = (u32)__popcll(bal);
        __syncthreads();
        u32 off = 0, tot = 0;
        for (int i = 0; i < 8; i++) { const u32 cn = L.wcnt[i]; if (i < wv) off += cn; tot += cn; }
        if (mt) elist[off + (u32)__popcll(bal & ((1ull << lane) - 1ull))] = (u32)e;
        __syncthreads();
        if (t < 256) {
          for (u32 i = 0; i < tot; i++) {
            const int ee = (int)elist[i];
            const int s = src[ee], d = dst[ee];
            float rwv = 0.f;
            for (int k = 0; k < 16; k++) rwv += bf1(ea[(size_t)ee * 16 + k]) * bf1(wrs[k * 256 + cch]);
            float tp = 0.f;
            for (int u = 0; u < 4; u++) {
              float a = 0.f;
              for (int v = 0; v < 4; v++) a += bf1(cg[(u * 4 + v) * 4 + w]) * bf1(sh[(size_t)ee * 4 + v]);
              tp += a * bf1(xs[((size_t)s * 4 + u) * 256 + cch]);
            }
            accs[(d - lo) * 256 + t] += tp * rwv;
          }
        }
        __syncthreads();
      }
      if (t < 256) {
        u16* ps = (u16*)part;
        for (int n = lo; n < hi; n++)
          ps[(((size_t)h * NN + n) * 4 + w) * 256 + cch] = (u16)(pack2(accs[(n - lo) * 256 + t], 0.f) & 0xFFFFu);
      }
      __syncthreads();
    }
  }
}

__device__ __forceinline__ u32 addbf2(u32 a, u32 b) { return pack2(bflo(a) + bflo(b), bfhi(a) + bfhi(b)); }
__device__ __forceinline__ u32x4 addbf8(u32x4 a, u32x4 b) {
  u32x4 r;
  r.x = addbf2(a.x, b.x); r.y = addbf2(a.y, b.y); r.z = addbf2(a.z, b.z); r.w = addbf2(a.w, b.w);
  return r;
}

__global__ __launch_bounds__(512, 2) void k3(const u32x4* __restrict__ part, const u32x4* __restrict__ wfrag,
                                             u32x4* __restrict__ out DBGARG) {
  __shared__ u32x4 tl[2][32 * 32];
  const int t = threadIdx.x, b = blockIdx.x;
  const int lane = t & 63, wv = t >> 6;
  const int row = t >> 4, c16 = t & 15;
  TS(12);
  const u32x4* p0 = part + row * 32 + c16;
  const u32x4* p1 = p0 + (size_t)NN * 128;
  const bool five = b < (1250 - 1024);
  u32x4 R[5][4];
  auto ld = [&](int s, int tile) {
    const size_t o = (size_t)tile * 1024;
    R[s][0] = p0[o]; R[s][1] = p0[o + 16]; R[s][2] = p1[o]; R[s][3] = p1[o + 16];
  };
  ld(0, b);
  __builtin_amdgcn_sched_barrier(0);
  u32x4 W[2][8];
#pragma unroll
  for (int p = 0; p < 2; p++)
#pragma unroll
    for (int ks = 0; ks < 8; ks++) W[p][ks] = wfrag[((wv * 2 + p) * 8 + ks) * 64 + lane];
  __builtin_amdgcn_sched_barrier(0);
  ld(1, b + 256);
  ld(2, b + 512);
  ld(3, b + 768);
  ld(4, five ? b + 1024 : b);
  __builtin_amdgcn_sched_barrier(0);
  const __amdgpu_buffer_rsrc_t ro_ = __builtin_amdgcn_make_buffer_rsrc((void*)out, 0, NN * 2048, 0x00020000);
  const int wo = row * 32 + (c16 ^ (row & 15));
  const int ro = (lane & 15) * 32;
  auto step = [&](int i) {
    const int s = i & 1;
    tl[s][wo] = addbf8(R[i][0], R[i][2]);
    tl[s][wo + 16] = addbf8(R[i][1], R[i][3]);
    __syncthreads();
    f32x4 acc[2][2];
#pragma unroll
    for (int rt = 0; rt < 2; rt++)
#pragma unroll
      for (int p = 0; p < 2; p++) acc[rt][p] = f32x4{0.f, 0.f, 0.f, 0.f};
#pragma unroll
    for (int ks = 0; ks < 8; ks++) {
      const int pc = ks * 4 + (lane >> 4);
      const int sw = (pc & 16) | ((pc ^ lane) & 15);
#pragma unroll
      for (int rt = 0; rt < 2; rt++) {
        const u32x4 bv = tl[s][rt * 512 + ro + sw];
#pragma unroll
        for (int p = 0; p < 2; p++)
          acc[rt][p] = __builtin_amdgcn_mfma_f32_16x16x32_bf16(__builtin_bit_cast(bf16x8, W[p][ks]),
                                                               __builtin_bit_cast(bf16x8, bv), acc[rt][p], 0, 0, 0);
      }
    }
    const int tile = b + 256 * i;
#pragma unroll
    for (int rt = 0; rt < 2; rt++) {
      u32x4 o;
      o.x = pack2(acc[rt][0][0], acc[rt][0][1]); o.y = pack2(acc[rt][0][2], acc[rt][0][3]);
      o.z = pack2(acc[rt][1][0], acc[rt][1][1]); o.w = pack2(acc[rt][1][2], acc[rt][1][3]);
      __builtin_amdgcn_raw_buffer_store_b128(o, ro_, (u32)(((tile * 32 + rt * 16 + (lane & 15)) * 32 + wv * 4 + (lane >> 4)) * 16), 0, AUX_K3);
    }
  };
  step(0);
  TS(13);
  step(1);
  step(2);
  step(3);
  if (five) step(4);
  TS(14);
}

extern "C" void kernel_launch(void* const* d_in, const int* in_sizes, int n_in, void* d_out, int out_size,
                              void* d_ws, size_t ws_size, hipStream_t stream) {
  const u32x4* x = (const u32x4*)d_in[0];
  const int* src = (const int*)d_in[1];
  const int* dst = (const int*)d_in[2];
  char* ws = (char*)d_ws;
  u32* meta = (u32*)(ws + WS_META);
  u32x4* wfrag = (u32x4*)(ws + WS_WFRAG);
  u32x4* rec = (u32x4*)(ws + WS_REC);
  u32x4* part = (u32x4*)(ws + WS_PART);
  u32* sink = (u32*)(ws + WS_SINK);
#define DBGP
  k1<<<NCH, 640, 0, stream>>>(x, src, dst, (const u32x2*)d_in[3], (const u32x4*)d_in[4], (const u32*)d_in[5],
                              (const u16*)d_in[7], meta, rec, wfrag, sink DBGP);
  k2<<<256, 512, 0, stream>>>(x, meta, rec, (const u32x4*)d_in[6], part, src, dst, (const u16*)d_in[3],
                              (const u16*)d_in[4], (const u16*)d_in[5], sink DBGP);
  k3<<<256, 512, 0, stream>>>(part, wfrag, (u32x4*)d_out DBGP);
}
```

```cpp
#include <hip/hip_runtime.h>
#include <stdint.h>

typedef unsigned int u32;
typedef unsigned short u16;
typedef unsigned char u8;
typedef u32 __attribute__((ext_vector_type(4))) u32x4;
typedef u32 __attribute__((ext_vector_type(2))) u32x2;
typedef float __attribute__((ext_vector_type(4))) f32x4;
typedef float __attribute__((ext_vector_type(2))) f32x2;
typedef short __attribute__((ext_vector_type(4))) s16x4;
typedef __bf16 __attribute__((ext_vector_type(8))) bf16x8;
typedef __bf16 __attribute__((ext_vector_type(2))) bf16x2;

#define AUX_K1 16
#define AUX_K2 0
#define AUX_ML 0
#define AUX_K3 16
#define PFG 64
#define PFS 250
#define PFQ 1
#define RX 0
#define PFT (128 / PFG)
#define PFSB 625
#define PF1 ((PFSB * PFT + 639) / 640)
#define PF2 (((625 - PFS) * PFT + 511) / 512)

constexpr int NN = 10000, NE = 160000, HALF = 5000;
constexpr int NB = 313;
constexpr int NCH = 256, CH = 625;
constexpr int CAPC = 32;
constexpr int CELLS = NCH * CAPC;
constexpr int CELLB = CAPC * 40;
constexpr u32 SENT = 0x003FFFFFu;
constexpr u32 OVF = 0x003FDFFFu;
constexpr int ITCAP = 400;

constexpr size_t WS_META = 0;
constexpr size_t WS_WFRAG = WS_META + (size_t)64 * CELLS * 4;
constexpr size_t WS_REC = WS_WFRAG + 131072;
constexpr size_t REC_BYTES = (size_t)64 * NCH * CELLB;
constexpr size_t WS_PART = WS_REC + REC_BYTES;
constexpr size_t PART_BYTES = (size_t)2 * NN * 2048;
constexpr size_t WS_SINK = WS_PART + PART_BYTES;

__device__ __forceinline__ u32 pack2(float a, float b) {
  f32x2 v = {a, b};
  bf16x2 r = __builtin_convertvector(v, bf16x2);
  return __builtin_bit_cast(u32, r);
}
__device__ __forceinline__ float bflo(u32 v) { return __uint_as_float(v << 16); }
__device__ __forceinline__ float bfhi(u32 v) { return __uint_as_float(v & 0xFFFF0000u); }
__device__ __forceinline__ float bf1(u16 v) { return __uint_as_float((u32)v << 16); }

template <int CTRL, int RM, bool BC>
__device__ __forceinline__ u32 dpp0(u32 v) { return (u32)__builtin_amdgcn_update_dpp(0, (int)v, CTRL, RM, 0xf, BC); }
__device__ __forceinline__ u32 wave_incl_scan(u32 v) {
  v += dpp0<0x111, 0xf, true>(v);
  v += dpp0<0x112, 0xf, true>(v);
  v += dpp0<0x114, 0xf, true>(v);
  v += dpp0<0x118, 0xf, true>(v);
  v += dpp0<0x142, 0xa, false>(v);
  v += dpp0<0x143, 0xc, false>(v);
  return v;
}
__device__ __forceinline__ u32 mbcnt64(u32 lo, u32 hi) {
  return __builtin_amdgcn_mbcnt_hi(hi, __builtin_amdgcn_mbcnt_lo(lo, 0u));
}

#define TS(i) do { } while (0)
#define DBGARG

struct K1Lds {
  u32x4 re[CH * 2 + 2];
  u32x2 rs[CH + 1];
  u32x4 ms[64 * 8];
  u32x4 cw[64];
  u32 dest[CH + 3];
};

__global__ __launch_bounds__(640) void k1(const u32x4* __restrict__ x, const int* __restrict__ src,
                                          const int* __restrict__ dst, const u32x2* __restrict__ sh,
                                          const u32x4* __restrict__ ea, const u16* __restrict__ wout,
                                          u32* __restrict__ meta, u32x4* __restrict__ rec,
                                          u32x4* __restrict__ wfrag DBGARG) {
  __shared__ K1Lds L;
  const int t = threadIdx.x, c = blockIdx.x;
  const int lane = t & 63, wv = __builtin_amdgcn_readfirstlane(t >> 6);
  TS(8);
  const bool act = t < CH;
  const int e = c * CH + (act ? t : 0);
  const int d = dst[e], s = src[e];
  const u32x2 shv = sh[e];
  const u32x4 ea0 = ea[2 * e], ea1 = ea[2 * e + 1];
  __builtin_amdgcn_sched_barrier(0);
  u32 pf[PF1];
  auto pfx = [&]() {
    __builtin_amdgcn_sched_barrier(0);
    const int role = (c & 7) ^ RX, j = c >> 3;
    const char* xb = (const char*)x + (size_t)(role & 1) * HALF * 2048 + (role >> 1) * 128;
    const int lim = ((((uintptr_t)xb >> 7) & 3) == 3 ? PFSB : PFS) * PFT;
#pragma unroll
    for (int k = 0; k < PF1; k++) {
      int i = t + 640 * k;
      i = i < lim ? i : lim - 1;
      pf[k] = *(const u32*)(xb + (u32)(j * 625 + i / PFT) * 512u + (i % PFT) * PFG);
    }
    __builtin_amdgcn_sched_barrier(0);
  };
  if (PFQ == 0) pfx();
  if (t < 512) L.ms[t] = u32x4{SENT, SENT, SENT, SENT};
  else if (t < 576) L.cw[t - 512] = u32x4{0, 0, 0, 0};
  __syncthreads();
  const int bin = d / NB, ld = d - bin * NB;
  if (PFQ == 1) { asm volatile("" ::"v"(bin)); pfx(); }
  const u32 hf = (s >= HALF ? 1u : 0u);
  const u32 hb = (u32)bin * 2 + hf;
  u32 r;
  {
    const unsigned long long am = __ballot(act);
    u32 lo = (u32)am, hi = (u32)(am >> 32);
#pragma unroll
    for (int bb = 0; bb < 6; bb++) {
      const bool bit = (hb >> bb) & 1u;
      const unsigned long long bal = __ballot(bit);
      const u32 bm = bit ? 0xFFFFFFFFu : 0u;
      lo &= ~((u32)bal ^ bm);
      hi &= ~((u32)(bal >> 32) ^ bm);
    }
    r = mbcnt64(lo, hi);
    if (act && r == 0) ((u8*)L.cw)[hb * 16 + wv] = (u8)(__popc(lo) + __popc(hi));
  }
  __syncthreads();
  TS(9);
  if (PFQ == 2) pfx();
  {
    const u32x4 cv = L.cw[lane];
    const u32 m0 = wv >= 4 ? 0x01010101u : (0x01010101u & ((1u << (8 * wv)) - 1u));
    const u32 m1 = wv >= 8 ? 0x01010101u : (wv <= 4 ? 0u : (0x01010101u & ((1u << (8 * (wv - 4))) - 1u)));
    const u32 m2 = wv <= 8 ? 0u : 0x00000001u;
    const u32 tot = __builtin_amdgcn_udot4(cv.x, 0x01010101u, __builtin_amdgcn_udot4(cv.y, 0x01010101u,
                    __builtin_amdgcn_udot4(cv.z, 0x01010101u, 0u, false), false), false);
    const u32 pre = __builtin_amdgcn_udot4(cv.x, m0, __builtin_amdgcn_udot4(cv.y, m1,
                    __builtin_amdgcn_udot4(cv.z, m2, 0u, false), false), false);
    const u32 inc = wave_incl_scan(tot);
    const u32 pk = (inc - tot + pre) | (pre << 10) | (tot > CAPC ? (1u << 20) : 0u);
    if (wv == 0 && tot > CAPC) {
#pragma unroll
      for (int i = 0; i < 8; i++) L.ms[lane * 8 + i] = u32x4{OVF, OVF, OVF, OVF};
    }
    const u32 v = (u32)__builtin_amdgcn_ds_bpermute((int)(hb * 4), (int)pk);
    if (act) {
      const u32 p = (v & 1023u) + r, rank = ((v >> 10) & 1023u) + r;
      L.re[p * 2] = ea0; L.re[p * 2 + 1] = ea1;
      L.rs[p] = shv;
      const bool ok = (v >> 20) == 0;
      L.dest[p] = ok ? ((hb * NCH + (u32)c) * CELLB + rank) : 0xF0000000u;
      if (ok) ((u32*)L.ms)[hb * CAPC + rank] = (u32)(s - (int)hf * HALF) | ((u32)ld << 13);
    }
  }
  if (PFQ == 3) pfx();
  __syncthreads();
  TS(10);
  {
    const __amdgpu_buffer_rsrc_t rr = __builtin_amdgcn_make_buffer_rsrc((void*)rec, 0, (int)REC_BYTES, 0x00020000);
    const __amdgpu_buffer_rsrc_t rm = __builtin_amdgcn_make_buffer_rsrc((void*)meta, 0, 64 * CELLS * 4, 0x00020000);
    if (t < 512)
      __builtin_amdgcn_raw_buffer_store_b128(L.ms[t], rm, ((u32)(t >> 3) * CELLS + (u32)c * CAPC) * 4u + (t & 7) * 16u, 0, AUX_K1);
#pragma unroll
    for (int k = 0; k < 2; k++) {
      const int i = t + 640 * k;
      if (i < CH * 2) {
        const u32 dd = L.dest[i >> 1];
        __builtin_amdgcn_raw_buffer_store_b128(L.re[i], rr, (dd & ~31u) + (dd & 31u) * 32u + (u32)(i & 1) * 16u, 0, AUX_K1);
      }
    }
    if (act) {
      const u32 dd = L.dest[t];
      __builtin_amdgcn_raw_buffer_store_b64(L.rs[t], rr, (dd & ~31u) + 1024u + (dd & 31u) * 8u, 0, AUX_K1);
    }
  }
  if (t >= 608) {
    const int item = c * 32 + (t - 608);
    const int tile = item >> 9, ks = (item >> 6) & 7, l = item & 63;
    const int rho = l & 15;
    const int dd = 32 * (tile >> 1) + 8 * (rho >> 2) + 4 * (tile & 1) + (rho & 3);
    const u16* wp = wout + (size_t)(32 * ks + 8 * (l >> 4)) * 256 + dd;
    u32x4 v;
    v.x = (u32)wp[0] | ((u32)wp[256] << 16);
    v.y = (u32)wp[512] | ((u32)wp[768] << 16);
    v.z = (u32)wp[1024] | ((u32)wp[1280] << 16);
    v.w = (u32)wp[1536] | ((u32)wp[1792] << 16);
    wfrag[item] = v;
  }
#pragma unroll
  for (int k = 0; k < PF1; k++) asm volatile("" ::"v"(pf[k]));
  TS(11);
}

constexpr int LCAP = 1280;
constexpr int ZREC = 2048 + LCAP;
struct K2Lds {
  u32x4 ea[2][ZREC + 1];
  u32x2 sh[ZREC + 2];
  u32x4 slot[ITCAP * 2];
  u32x4 stage[136];
  u32x4 cnt[320];
  u32 dh[64];
  u32 wt[8][2][40];
  u16 sorted[320];
  u32 flag;
  u32 wcnt[8];
};

__device__ __forceinline__ u32 slot_index(u32 it, u32 g) { return ((it >> 2) << 5) + (g << 2) + (it & 3); }

#define XOFF(o) (o)
#define POFF(o) (o)
#define K3D 2
#define K3O 0
#define RLA 1
#define RL0 2
#define RL1 4
#define RL2 5
#define RL3 7
#define ORDV 0, 1, 4, 2, 3, 5, 6, 7
#define PFP 3
#define SR0 0
#define SR1 2
#define SR2 5
#define BTOF(k, w) (8 * (k) + ((k) ? 7 - (w) : (w)))
#define SPR 2
constexpr int ORD[8] = {ORDV};
constexpr int NG = 8;

__global__ __launch_bounds__(512, 2) void k2(const u32x4* __restrict__ x, const u32* __restrict__ meta,
                                             const u32x4* __restrict__ rec, const u32x4* __restrict__ wr,
                                             u32x4* __restrict__ part, const int* __restrict__ src,
                                             const int* __restrict__ dst, const u16* __restrict__ sh,
                                             const u16* __restrict__ ea, const u16* __restrict__ cg DBGARG) {
  __shared__ K2Lds L;
  const int t = threadIdx.x, b = blockIdx.x;
  const int lane = t & 63, wv = __builtin_amdgcn_readfirstlane(t >> 6);
  const int sl = ((b & 7) ^ RX) >> 1, h = (b ^ RX) & 1, bin = b >> 3;
  const int hb = bin * 2 + h;
  const int nnode = (bin == 31) ? (NN - 31 * NB) : NB;
  TS(0);
  u32 md[NG / 2];
  u32x4 m[3];
  u32 pid[3];
  u32x4 R0[NG], R1[NG];
  u32x2 R2[NG];
  u32 ra[NG], rb[NG];
  const __amdgpu_buffer_rsrc_t rr = __builtin_amdgcn_make_buffer_rsrc(
      (void*)((const char*)rec + (size_t)hb * NCH * CELLB), 0, NCH * CELLB, 0x00020000);
  auto ldrec = [&](int k0, int k1) {
    __builtin_amdgcn_sched_barrier(0);
#pragma unroll
    for (int kk = k0; kk < k1; kk++) {
      const int k = ORD[kk];
      R0[k] = __builtin_amdgcn_raw_buffer_load_b128(rr, ra[k], 0, AUX_ML);
      R1[k] = __builtin_amdgcn_raw_buffer_load_b128(rr, ra[k] + 16u, 0, AUX_ML);
      R2[k] = __builtin_amdgcn_raw_buffer_load_b64(rr, rb[k], 0, AUX_ML);
    }
    __builtin_amdgcn_sched_barrier(0);
  };
  {
    const __amdgpu_buffer_rsrc_t rm =
        __builtin_amdgcn_make_buffer_rsrc((void*)(meta + (size_t)hb * CELLS), 0, CELLS * 4, 0x00020000);
#pragma unroll
    for (int k = 0; k < NG / 2; k++) {
      const u32 cell = (u32)(wv * 32 + k * 8 + (lane >> 3));
      md[k] = __builtin_amdgcn_raw_buffer_load_b32(rm, (cell * 32u + (lane & 7)) * 4u, 0, AUX_ML);
      ra[k] = cell * CELLB + (lane & 7) * 32u;
      rb[k] = cell * CELLB + 1024u + (lane & 7) * 8u;
    }
#pragma unroll
    for (int k = 0; k < 3; k++) {
      const u32 i = (u32)(k * 64 + lane), cl = i / 6u;
      pid[k] = ((u32)wv * 32u + cl) * 8u + 2u + (i - cl * 6u);
      m[k] = __builtin_amdgcn_raw_buffer_load_b128(rm, pid[k] * 16u, 0, AUX_ML);
    }
  }
  u32x4 stg = u32x4{0, 0, 0, 0};
  if (t < 128) stg = wr[(t >> 3) * 32 + sl * 8 + (t & 7)];
  else if (t < 136) stg = ((const u32x4*)cg)[t - 128];
  const char* xb = (const char*)x + (size_t)h * HALF * 2048 + sl * 128;
  u32 pf[PF2];
  auto pfx = [&]() {
    __builtin_amdgcn_sched_barrier(0);
    const int beg = ((((uintptr_t)xb >> 7) & 3) == 3 ? PFSB : PFS) * PFT;
#pragma unroll
    for (int k = 0; k < PF2; k++) {
      int i = beg + t + 512 * k;
      i = i < 625 * PFT ? i : 625 * PFT - 1;
      pf[k] = *(const u32*)(xb + (u32)(bin * 625 + i / PFT) * 512u + (i % PFT) * PFG);
    }
    __builtin_amdgcn_sched_barrier(0);
  };
  if (PFP == 0) pfx();
  ldrec(0, RLA);
  {
    constexpr u32 PADS = 0xFFFFu | ((u32)ZREC << 16);
    L.slot[t] = u32x4{PADS, PADS, PADS, PADS};
    if (t + 512 < ITCAP * 2) L.slot[t + 512] = u32x4{PADS, PADS, PADS, PADS};
    if (t >= 510) L.ea[t - 510][ZREC] = u32x4{0, 0, 0, 0};
    if (t == 509) L.sh[ZREC] = u32x2{0, 0};
  }
  if (t < 320) L.cnt[t] = u32x4{0, 0, 0, 0};
  else if (t < 384) L.dh[t - 320] = 0;
  else if (t == 384) L.flag = 0;
  ldrec(RLA, RL0);
  if (PFP == 1) pfx();
  __syncthreads();
  u32x4* list = &L.ea[0][2048 + wv * 64];
  u32 np = 0;
#pragma unroll
  for (int k = 0; k < 3; k++) {
    const bool ne = m[k].x != SENT;
    const unsigned long long bal = __ballot(ne);
    u32x4 v = m[k];
    v.x |= (pid[k] & 1023u) << 22;
    v.y |= (pid[k] >> 10) << 22;
    const u32 idx = np + mbcnt64((u32)bal, (u32)(bal >> 32));
    if (ne && idx < NG * 8) list[idx] = v;
    np += (u32)__popcll(bal);
  }
  TS(1);
  const u32 ns = np * 4;
  u32 ev[NG], el[NG], rk[NG];
  const u32 csh = (wv & 1) * 16;
  {
    bool bad = ns > NG * 32;
#pragma unroll
    for (int k = NG / 2; k < NG; k++) {
      const u32 i = (k - NG / 2) * 64 + lane;
      const u32 v = ((const u32*)list)[i];
      const u32 pd = (dpp0<0x00, 0xf, false>(v) >> 22) | ((dpp0<0x55, 0xf, false>(v) >> 22) << 10);
      const u32 ld = (i < ns) ? ((v >> 13) & 511u) : 511u;
      el[k] = ld;
      ev[k] = v & 0x1FFFu;
      const u32 rkc = (pd & 7u) * 4u + (lane & 3);
      const u32 cb = ld < 320 ? (pd >> 3) * CELLB : 0xF0000000u;
      ra[k] = cb + rkc * 32u;
      rb[k] = cb + rkc * 8u + 1024u;
    }
    ldrec(RL0, RL1);
#pragma unroll
    for (int k = 0; k < NG / 2; k++) {
      el[k] = (md[k] >> 13) & 511u;
      ev[k] = md[k] & 0x1FFFu;
      bad |= (el[k] == 510);
    }
#pragma unroll
    for (int k = 0; k < NG; k++) {
      rk[k] = 0;
      if (el[k] < 320) rk[k] = atomicAdd(&((u32*)L.cnt)[el[k] * 4 + (wv >> 1)], 1u << csh);
    }
    if (bad) L.flag = 1;
    if (t < 136) L.stage[t] = stg;
    if (lane == 0) L.wcnt[wv] = ns;
  }
  ldrec(RL1, RL2);
  if (PFP == 2) pfx();
  __syncthreads();
  TS(2);
  u32 lbase;
  {
    const u32 cw = L.wcnt[lane & 7];
    u32 tot = cw;
    tot += dpp0<0xB1, 0xf, false>(tot);
    tot += dpp0<0x4E, 0xf, false>(tot);
    tot += dpp0<0x141, 0xf, false>(tot);
    u32 pw = (lane & 7) < wv ? cw : 0u;
    pw += dpp0<0xB1, 0xf, false>(pw);
    pw += dpp0<0x4E, 0xf, false>(pw);
    pw += dpp0<0x141, 0xf, false>(pw);
    lbase = 2048u + pw + lane;
    if (tot > LCAP) L.flag = 1;
  }
  auto strec = [&](int k0, int k1) {
    __builtin_amdgcn_sched_barrier(0);
#pragma unroll
    for (int kk = k0; kk < k1; kk++) {
      const int k = ORD[kk];
      const u32 ri = k < NG / 2 ? (u32)(wv * 256 + k * 64 + lane) : lbase + (k - NG / 2) * 64;
      if (k < NG / 2 || (el[k] < 320 && ri < ZREC)) {
        L.ea[0][ri] = R0[k];
        L.ea[1][ri] = R1[k];
        L.sh[ri] = R2[k];
      }
    }
    __builtin_amdgcn_sched_barrier(0);
  };
  u32 mydeg = 0, r2 = 0, pre[8];
  if (t < 320) {
    const u32x4 cv = L.cnt[t];
#pragma unroll
    for (int w = 0; w < 8; w++) {
      pre[w] = mydeg;
      mydeg += (cv[w >> 1] >> ((w & 1) * 16)) & 0xFFFFu;
    }
    if (mydeg > 63) { L.flag = 1; mydeg = 63; }
    r2 = atomicAdd(&L.dh[63 - mydeg], 1u);
  }
  ldrec(RL2, RL3);
  if (PFP == 3) pfx();
  strec(0, SR0);
  __syncthreads();
  TS(3);
  u32 hst, binc, blen_l;
  bool slow;
  {
    const u32 hv = L.dh[lane];
    const u32 hinc = wave_incl_scan(hv);
    hst = hinc - hv;
    for (u32 bt = (hst + 7) >> 3; bt < ((hinc + 7) >> 3); bt++) L.wt[wv][0][bt] = (lane == 63) ? 1u : (u32)(63 - lane);
    const int w = lane >> 3, k = lane & 7;
    const int bt = BTOF(k, w) % 40;
    u32 len = L.wt[wv][0][bt];
    if (k >= 5) len = 0;
    u32 tot = len;
    tot += dpp0<0xB1, 0xf, false>(tot);
    tot += dpp0<0x4E, 0xf, false>(tot);
    tot += dpp0<0x141, 0xf, false>(tot);
    if (k == 5) len = (4 - (tot & 3)) & 3;
    binc = wave_incl_scan(len);
    blen_l = len;
    if (k < 5) L.wt[wv][1][bt] = binc - len;
    slow = (L.flag != 0) || ((u32)__builtin_amdgcn_readlane((int)binc, 63) > ITCAP);
  }
  if (!slow) {
    const u32 mypos = (u32)__builtin_amdgcn_ds_bpermute((int)((63 - mydeg) * 4), (int)hst) + r2;
    if (t < 320) {
      const u32 o = L.wt[wv][1][mypos >> 3] | ((mypos & 7) << 10);
      u32x4 pw;
#pragma unroll
      for (int i = 0; i < 4; i++) pw[i] = (o + pre[2 * i]) | ((o + pre[2 * i + 1]) << 16);
      L.cnt[t] = pw;
      L.sorted[mypos] = (u16)t;
    }
    ldrec(RL3, NG);
    strec(SR0, SR1);
    __syncthreads();
#pragma unroll
    for (int k = 0; k < NG; k++) {
      const u32 pw = ((const u16*)L.cnt)[min(el[k], 319u) * 8 + wv];
      rk[k] = slot_index((pw & 1023u) + ((rk[k] >> csh) & 0xFFFFu), pw >> 10);
    }
#pragma unroll
    for (int k = 0; k < NG; k++)
      if (el[k] < 320)
        ((u32*)L.slot)[rk[k]] = ev[k] | ((k < NG / 2 ? (u32)(wv * 256 + k * 64 + lane) : lbase + (k - NG / 2) * 64) << 16);
    const int g = lane >> 3, li = lane & 7;
    const u32 it0 = (u32)__builtin_amdgcn_readlane((int)(binc - blen_l), wv * 8);
    const u32 q0 = it0 >> 2;
    const int nq = (int)(((u32)__builtin_amdgcn_readlane((int)binc, wv * 8 + 5) - it0) >> 2);
    u32x4 Bj[8];
    {
      u32x4 wrow[8];
#pragma unroll
      for (int jj = 0; jj < 8; jj++) wrow[jj] = L.stage[(((lane >> 4) & 1) * 8 + jj) * 8 + (lane & 7)];
      const bool nz = ((lane >> 5) == ((lane >> 3) & 1));
#pragma unroll
      for (int j = 0; j < 8; j++) {
#pragma unroll
        for (int p = 0; p < 4; p++) {
          const u32 lo = wrow[2 * p][j >> 1], hi = wrow[2 * p + 1][j >> 1];
          const u32 v = (j & 1) ? __builtin_amdgcn_perm(hi, lo, 0x07060302u) : __builtin_amdgcn_perm(hi, lo, 0x05040100u);
          Bj[j][p] = nz ? v : 0u;
        }
      }
    }
    u32x2 cgB[4];
    u16 cgr[16];
#pragma unroll
    for (int i = 0; i < 16; i++) cgr[i] = ((const u16*)&L.stage[128])[i * 4 + (lane & 3)];
#pragma unroll
    for (int u = 0; u < 4; u++) {
      cgB[u].x = (u32)cgr[u * 4] | ((u32)cgr[u * 4 + 1] << 16);
      cgB[u].y = (u32)cgr[u * 4 + 2] | ((u32)cgr[u * 4 + 3] << 16);
    }
    strec(SR1, SR2);
    __syncthreads();
    TS(4);
    const __amdgpu_buffer_rsrc_t rx =
        __builtin_amdgcn_make_buffer_rsrc((void*)xb, 0, HALF * 2048 - sl * 128, 0x00020000);
    const u32 loff = li * 16;
    u32x4 X[4][4];
    auto issue_x = [&](int r, u32 s16) {
      const u32 off = s16 * 2048u + loff;
#pragma unroll
      for (int u = 0; u < 4; u++) X[r][u] = __builtin_amdgcn_raw_buffer_load_b128(rx, off + u * 512, 0, 0);
    };
    auto s16of = [&](u32x4 en, int r) { return en[r] & 0xFFFFu; };
    {
      const u32x4 en = L.slot[q0 * 8 + g];
#pragma unroll
      for (int r = 0; r < 4; r++) { issue_x(r, s16of(en, r)); __builtin_amdgcn_sched_barrier(0); }
    }
    TS(7);
    strec(SR2, NG);
    TS(15);
#pragma unroll
    for (int k = 0; k < PF2; k++) asm volatile("" ::"v"(pf[k]));
    __syncthreads();
    TS(6);
    const __amdgpu_buffer_rsrc_t rp = __builtin_amdgcn_make_buffer_rsrc((void*)part, 0, (int)PART_BYTES, 0x00020000);
    const u32 es = (2 * ((lane & 15) >> 2) + (lane >> 5)) * 4 + (lane & 3);
    const u32x4* eap = L.ea[(lane >> 4) & 1];
    const u16* iep = (const u16*)L.slot + (q0 * 32 + es) * 2 + 1;
    const u16* isp = (const u16*)L.slot + (q0 * 32 + g * 4 + (lane & 3)) * 2 + 1;
    const u32x4* srp = L.slot + (q0 + 1) * 8 + g;
    f32x4 acc[8];
#pragma unroll
    for (int j = 0; j < 8; j++) acc[j] = f32x4{0.f, 0.f, 0.f, 0.f};
    u32x4 eav = eap[iep[0]];
    u32x2 shv = L.sh[isp[0]];
    u32 ie = iep[64], is = isp[64];
    u32 it = it0;
    int kb = 0;
    u32 te = (u32)__builtin_amdgcn_readlane((int)binc, wv * 8);
    u32 ndc = L.sorted[wv * 8 + g];
    const u32 pbase = (u32)(h * NN + bin * NB) * 2048u + sl * 128 + li * 16;
#define SB __builtin_amdgcn_sched_barrier(0)
    auto quad = [&](int Q, auto LOADT) {
      constexpr bool LOAD = decltype(LOADT)::value;
      u32x4 en;
      if constexpr (LOAD) en = srp[Q * 8];
      u32x2 av[4];
      f32x4 Du[4], rw[8];
      u32 xo[4];
#pragma unroll
      for (int u = 0; u < 4; u++)
        Du[u] = __builtin_amdgcn_mfma_f32_4x4x4bf16_1k(__builtin_bit_cast(s16x4, shv), __builtin_bit_cast(s16x4, cgB[u]),
                                                       f32x4{0.f, 0.f, 0.f, 0.f}, 0, 0, 0);
      SB;
#pragma unroll
      for (int j = 0; j < 8; j++) {
        rw[j] = __builtin_amdgcn_mfma_f32_16x16x32_bf16(__builtin_bit_cast(bf16x8, eav), __builtin_bit_cast(bf16x8, Bj[j]),
                                                        f32x4{0.f, 0.f, 0.f, 0.f}, 0, 0, 0);
        if (j >= 1 && j <= 4) {
          av[j - 1].x = pack2(Du[0][j - 1], Du[1][j - 1]);
          av[j - 1].y = pack2(Du[2][j - 1], Du[3][j - 1]);
        }
        if constexpr (LOAD) {
          if (j == 5 || j == 6) {
            xo[2 * j - 10] = s16of(en, 2 * j - 10) * 2048u + loff;
            xo[2 * j - 9] = s16of(en, 2 * j - 9) * 2048u + loff;
          }
        }
        SB;
      }
      if constexpr (LOAD) {
        eav = eap[ie];
        shv = L.sh[is];
        ie = iep[(Q + 2) * 64];
        is = isp[(Q + 2) * 64];
      }
#pragma unroll
      for (int r = 0; r < 4; r++) {
        u32x2 Bp[8];
        f32x4 D[8];
        auto prm = [&](int j) {
          const u32 sel = (j & 1) ? 0x07060302u : 0x05040100u;
          Bp[j].x = __builtin_amdgcn_perm(X[r][1][j >> 1], X[r][0][j >> 1], sel);
          Bp[j].y = __builtin_amdgcn_perm(X[r][3][j >> 1], X[r][2][j >> 1], sel);
        };
        auto ldx = [&](int u) { X[r][u] = __builtin_amdgcn_raw_buffer_load_b128(rx, XOFF(xo[r]) + u * 512, 0, 0); };
        prm(0); prm(1);
        SB;
#pragma unroll
        for (int j = 0; j < 8; j++) {
          D[j] = __builtin_amdgcn_mfma_f32_4x4x4bf16_1k(__builtin_bit_cast(s16x4, av[r]), __builtin_bit_cast(s16x4, Bp[j]),
                                                        f32x4{0.f, 0.f, 0.f, 0.f}, 0, 0, 0);
          if (j < 6) prm(j + 2);
          if constexpr (LOAD) {
            if (j == 6) { ldx(0); ldx(1); }
            if (j == 7) { ldx(2); ldx(3); }
          }
          SB;
        }
#pragma unroll
        for (int j = 0; j < 8; j++) acc[j] += D[j] * rw[j][r];
        SB;
        it++;
        if (it == te) {
          const u32 n = ndc < (u32)nnode ? pbase + ndc * 2048u : 0xF0000000u;
#pragma unroll
          for (int w = 0; w < 4; w++) {
            u32x4 o;
            o.x = pack2(acc[0][w], acc[1][w]); o.y = pack2(acc[2][w], acc[3][w]);
            o.z = pack2(acc[4][w], acc[5][w]); o.w = pack2(acc[6][w], acc[7][w]);
            __builtin_amdgcn_raw_buffer_store_b128(o, rp, POFF(n) + w * 512, 0, AUX_K2);
          }
#pragma unroll
          for (int j = 0; j < 8; j++) {
            f32x2 z0, z1;
            asm volatile("v_mov_b64 %0, 0" : "=v"(z0));
            asm volatile("v_mov_b64 %0, 0" : "=v"(z1));
            acc[j] = f32x4{z0.x, z0.y, z1.x, z1.y};
          }
          kb++;
          te = kb < 5 ? (u32)__builtin_amdgcn_readlane((int)binc, wv * 8 + kb) : 0xFFFFFFFFu;
          const int kn = kb < 5 ? kb : 4;
          ndc = L.sorted[BTOF(kn, wv) * 8 + g];
        }
      }
    };
    for (int Q = 0; Q < nq - 1; Q++) quad(Q, std::true_type{});
    quad(nq - 1, std::false_type{});
    return;
  }

  {
    __syncthreads();
    float* accs = (float*)&L.ea[0][0];
    u32* elist = (u32*)&L.ea[0][2048];
    const int w = (t >> 6) & 3, cch = sl * 64 + (t & 63);
    const u16* xs = (const u16*)x;
    const u16* wrs = (const u16*)wr;
    for (int n0 = 0; n0 < nnode; n0 += 32) {
      const int lo = bin * NB + n0;
      const int hi = min(lo + 32, bin * NB + nnode);
      for (int i = t; i < 32 * 256; i += 512) accs[i] = 0.f;
      for (int e0 = 0; e0 < NE; e0 += 512) {
        const int e = e0 + t;
        bool mt = false;
        if (e < NE) {
          const int d = dst[e], s = src[e];
          mt = (d >= lo) && (d < hi) && ((s >= HALF ? 1 : 0) == h);
        }
        const unsigned long long bal = __ballot(mt);
        if (lane == 0) L.wcnt[wv] = (u32)__popcll(bal);
        __syncthreads();
        u32 off = 0, tot = 0;
        for (int i = 0; i < 8; i++) { const u32 cn = L.wcnt[i]; if (i < wv) off += cn; tot += cn; }
        if (mt) elist[off + (u32)__popcll(bal & ((1ull << lane) - 1ull))] = (u32)e;
        __syncthreads();
        if (t < 256) {
          for (u32 i = 0; i < tot; i++) {
            const int ee = (int)elist[i];
            const int s = src[ee], d = dst[ee];
            float rwv = 0.f;
            for (int k = 0; k < 16; k++) rwv += bf1(ea[(size_t)ee * 16 + k]) * bf1(wrs[k * 256 + cch]);
            float tp = 0.f;
            for (int u = 0; u < 4; u++) {
              float a = 0.f;
              for (int v = 0; v < 4; v++) a += bf1(cg[(u * 4 + v) * 4 + w]) * bf1(sh[(size_t)ee * 4 + v]);
              tp += a * bf1(xs[((size_t)s * 4 + u) * 256 + cch]);
            }
            accs[(d - lo) * 256 + t] += tp * rwv;
          }
        }
        __syncthreads();
      }
      if (t < 256) {
        u16* ps = (u16*)part;
        for (int n = lo; n < hi; n++)
          ps[(((size_t)h * NN + n) * 4 + w) * 256 + cch] = (u16)(pack2(accs[(n - lo) * 256 + t], 0.f) & 0xFFFFu);
      }
      __syncthreads();
    }
  }
}

__device__ __forceinline__ u32 addbf2(u32 a, u32 b) { return pack2(bflo(a) + bflo(b), bfhi(a) + bfhi(b)); }
__device__ __forceinline__ u32x4 addbf8(u32x4 a, u32x4 b) {
  u32x4 r;
  r.x = addbf2(a.x, b.x); r.y = addbf2(a.y, b.y); r.z = addbf2(a.z, b.z); r.w = addbf2(a.w, b.w);
  return r;
}

__global__ __launch_bounds__(512, 2) void k3(const u32x4* __restrict__ part, const u32x4* __restrict__ wfrag,
                                             u32x4* __restrict__ out DBGARG) {
  __shared__ u32x4 tl[2][32 * 32];
  const int t = threadIdx.x, b = blockIdx.x;
  const int lane = t & 63, wv = t >> 6;
  const int row = t >> 4, c16 = t & 15;
  TS(12);
  const u32x4* p0 = part + row * 32 + c16;
  const u32x4* p1 = p0 + (size_t)NN * 128;
  const bool five = b < (1250 - 1024);
  u32x4 R[5][4];
  auto ld = [&](int s, int tile) {
    const size_t o = (size_t)tile * 1024;
    R[s][0] = p0[o]; R[s][1] = p0[o + 16]; R[s][2] = p1[o]; R[s][3] = p1[o + 16];
  };
  u32x4 W[2][8];
  auto ldw = [&]() {
    __builtin_amdgcn_sched_barrier(0);
#pragma unroll
    for (int ks = 0; ks < 8; ks++)
#pragma unroll
      for (int p = 0; p < 2; p++) W[p][ks] = wfrag[((wv * 2 + p) * 8 + ks) * 64 + lane];
    __builtin_amdgcn_sched_barrier(0);
  };
  ld(0, b);
  if (K3O == 0) ldw();
  ld(1, b + 256);
  if (K3O == 1) ldw();
  __builtin_amdgcn_sched_barrier(0);
  const __amdgpu_buffer_rsrc_t ro_ = __builtin_amdgcn_make_buffer_rsrc((void*)out, 0, NN * 2048, 0x00020000);
  const int wo = row * 32 + (c16 ^ (row & 15));
  const int ro = (lane & 15) * 32;
  auto step = [&](int i) {
    const int s = i & 1;
    tl[s][wo] = addbf8(R[i][0], R[i][2]);
    tl[s][wo + 16] = addbf8(R[i][1], R[i][3]);
    __builtin_amdgcn_sched_barrier(0);
    if (i + K3D < 4) ld(i + K3D, b + 256 * (i + K3D));
    else if (i + K3D == 4) ld(4, five ? b + 1024 : b);
    __builtin_amdgcn_sched_barrier(0);
    __syncthreads();
    f32x4 acc[2][2];
#pragma unroll
    for (int rt = 0; rt < 2; rt++)
#pragma unroll
      for (int p = 0; p < 2; p++) acc[rt][p] = f32x4{0.f, 0.f, 0.f, 0.f};
#pragma unroll
    for (int ks = 0; ks < 8; ks++) {
      const int pc = ks * 4 + (lane >> 4);
      const int sw = (pc & 16) | ((pc ^ lane) & 15);
#pragma unroll
      for (int rt = 0; rt < 2; rt++) {
        const u32x4 bv = tl[s][rt * 512 + ro + sw];
#pragma unroll
        for (int p = 0; p < 2; p++)
          acc[rt][p] = __builtin_amdgcn_mfma_f32_16x16x32_bf16(__builtin_bit_cast(bf16x8, W[p][ks]),
                                                               __builtin_bit_cast(bf16x8, bv), acc[rt][p], 0, 0, 0);
      }
    }
    const int tile = b + 256 * i;
#pragma unroll
    for (int rt = 0; rt < 2; rt++) {
      u32x4 o;
      o.x = pack2(acc[rt][0][0], acc[rt][0][1]); o.y = pack2(acc[rt][0][2], acc[rt][0][3]);
      o.z = pack2(acc[rt][1][0], acc[rt][1][1]); o.w = pack2(acc[rt][1][2], acc[rt][1][3]);
      __builtin_amdgcn_raw_buffer_store_b128(o, ro_, (u32)(((tile * 32 + rt * 16 + (lane & 15)) * 32 + wv * 4 + (lane >> 4)) * 16), 0, AUX_K3);
    }
  };
  step(0);
  TS(13);
  step(1);
  step(2);
  step(3);
  if (five) step(4);
  TS(14);
}

extern "C" void kernel_launch(void* const* d_in, const int* in_sizes, int n_in, void* d_out, int out_size,
                              void* d_ws, size_t ws_size, hipStream_t stream) {
  const u32x4* x = (const u32x4*)d_in[0];
  const int* src = (const int*)d_in[1];
  const int* dst = (const int*)d_in[2];
  char* ws = (char*)d_ws;
  u32* meta = (u32*)(ws + WS_META);
  u32x4* wfrag = (u32x4*)(ws + WS_WFRAG);
  u32x4* rec = (u32x4*)(ws + WS_REC);
  u32x4* part = (u32x4*)(ws + WS_PART);
#define DBGP
  k1<<<NCH, 640, 0, stream>>>(x, src, dst, (const u32x2*)d_in[3], (const u32x4*)d_in[4], (const u16*)d_in[7], meta,
                              rec, wfrag DBGP);
  k2<<<256, 512, 0, stream>>>(x, meta, rec, (const u32x4*)d_in[6], part, src, dst, (const u16*)d_in[3],
                              (const u16*)d_in[4], (const u16*)d_in[5] DBGP);
  k3<<<256, 512, 0, stream>>>(part, wfrag, (u32x4*)d_out DBGP);
}
```

```cpp
#include <hip/hip_runtime.h>
#include <stdint.h>

typedef unsigned int u32;
typedef unsigned short u16;
typedef unsigned char u8;
typedef u32 __attribute__((ext_vector_type(4))) u32x4;
typedef u32 __attribute__((ext_vector_type(2))) u32x2;
typedef float __attribute__((ext_vector_type(4))) f32x4;
typedef float __attribute__((ext_vector_type(2))) f32x2;
typedef short __attribute__((ext_vector_type(4))) s16x4;
typedef __bf16 __attribute__((ext_vector_type(8))) bf16x8;
typedef __bf16 __attribute__((ext_vector_type(2))) bf16x2;

#define AUX_K1 16
#define AUX_K2 0
#define AUX_ML 0
#define AUX_K3 16
#define PFG 64
#define PFS 300
#define PFT (128 / PFG)
#define PF1 ((PFS * PFT + 639) / 640)
#define PF2 (((625 - PFS) * PFT + 511) / 512)

constexpr int NN = 10000, NE = 160000, HALF = 5000;
constexpr int NB = 313;
constexpr int NCH = 256, CH = 625;
constexpr int CAPC = 32;
constexpr int CELLS = NCH * CAPC;
constexpr int CELLB = CAPC * 40;
constexpr u32 SENT = 0x003FFFFFu;
constexpr u32 OVF = 0x003FDFFFu;
constexpr int ITCAP = 400;

constexpr size_t WS_META = 0;
constexpr size_t WS_WFRAG = WS_META + (size_t)64 * CELLS * 4;
constexpr size_t WS_REC = WS_WFRAG + 131072;
constexpr size_t REC_BYTES = (size_t)64 * NCH * CELLB;
constexpr size_t WS_PART = WS_REC + REC_BYTES;
constexpr size_t PART_BYTES = (size_t)2 * NN * 2048;
constexpr size_t WS_SINK = WS_PART + PART_BYTES;

__device__ __forceinline__ u32 pack2(float a, float b) {
  f32x2 v = {a, b};
  bf16x2 r = __builtin_convertvector(v, bf16x2);
  return __builtin_bit_cast(u32, r);
}
__device__ __forceinline__ float bflo(u32 v) { return __uint_as_float(v << 16); }
__device__ __forceinline__ float bfhi(u32 v) { return __uint_as_float(v & 0xFFFF0000u); }
__device__ __forceinline__ float bf1(u16 v) { return __uint_as_float((u32)v << 16); }

template <int CTRL, int RM, bool BC>
__device__ __forceinline__ u32 dpp0(u32 v) { return (u32)__builtin_amdgcn_update_dpp(0, (int)v, CTRL, RM, 0xf, BC); }
__device__ __forceinline__ u32 wave_incl_scan(u32 v) {
  v += dpp0<0x111, 0xf, true>(v);
  v += dpp0<0x112, 0xf, true>(v);
  v += dpp0<0x114, 0xf, true>(v);
  v += dpp0<0x118, 0xf, true>(v);
  v += dpp0<0x142, 0xa, false>(v);
  v += dpp0<0x143, 0xc, false>(v);
  return v;
}
__device__ __forceinline__ u32 mbcnt64(u32 lo, u32 hi) {
  return __builtin_amdgcn_mbcnt_hi(hi, __builtin_amdgcn_mbcnt_lo(lo, 0u));
}

#define TS(i) do { } while (0)
#define DBGARG

struct K1Lds {
  u32x4 re[CH * 2 + 2];
  u32x2 rs[CH + 1];
  u32x4 ms[64 * 8];
  u32x4 cw[64];
  u32 dest[CH + 3];
};

__global__ __launch_bounds__(640) void k1(const u32x4* __restrict__ x, const int* __restrict__ src,
                                          const int* __restrict__ dst, const u32x2* __restrict__ sh,
                                          const u32x4* __restrict__ ea, const u16* __restrict__ wout,
                                          u32* __restrict__ meta, u32x4* __restrict__ rec,
                                          u32x4* __restrict__ wfrag DBGARG) {
  __shared__ K1Lds L;
  const int t = threadIdx.x, c = blockIdx.x;
  const int lane = t & 63, wv = __builtin_amdgcn_readfirstlane(t >> 6);
  TS(8);
  const bool act = t < CH;
  const int e = c * CH + (act ? t : 0);
  const int d = dst[e], s = src[e];
  const u32x2 shv = sh[e];
  const u32x4 ea0 = ea[2 * e], ea1 = ea[2 * e + 1];
  __builtin_amdgcn_sched_barrier(0);
  u32 pf[PF1];
  {
    const int role = c & 7, j = c >> 3;
    const char* xb = (const char*)x + (size_t)(role & 1) * HALF * 2048 + (role >> 1) * 128;
#pragma unroll
    for (int k = 0; k < PF1; k++) {
      int i = t + 640 * k;
      i = i < PFS * PFT ? i : PFS * PFT - 1;
      pf[k] = *(const u32*)(xb + (u32)(j * 625 + i / PFT) * 512u + (i % PFT) * PFG);
    }
  }
  __builtin_amdgcn_sched_barrier(0);
  if (t < 512) L.ms[t] = u32x4{SENT, SENT, SENT, SENT};
  else if (t < 576) L.cw[t - 512] = u32x4{0, 0, 0, 0};
  __syncthreads();
  const int bin = d / NB, ld = d - bin * NB;
  const u32 hf = (s >= HALF ? 1u : 0u);
  const u32 hb = (u32)bin * 2 + hf;
  u32 r;
  {
    const unsigned long long am = __ballot(act);
    u32 lo = (u32)am, hi = (u32)(am >> 32);
#pragma unroll
    for (int bb = 0; bb < 6; bb++) {
      const bool bit = (hb >> bb) & 1u;
      const unsigned long long bal = __ballot(bit);
      const u32 bm = bit ? 0xFFFFFFFFu : 0u;
      lo &= ~((u32)bal ^ bm);
      hi &= ~((u32)(bal >> 32) ^ bm);
    }
    r = mbcnt64(lo, hi);
    if (act && r == 0) ((u8*)L.cw)[hb * 16 + wv] = (u8)(__popc(lo) + __popc(hi));
  }
  __syncthreads();
  TS(9);
  {
    const u32x4 cv = L.cw[lane];
    const u32 m0 = wv >= 4 ? 0x01010101u : (0x01010101u & ((1u << (8 * wv)) - 1u));
    const u32 m1 = wv >= 8 ? 0x01010101u : (wv <= 4 ? 0u : (0x01010101u & ((1u << (8 * (wv - 4))) - 1u)));
    const u32 m2 = wv <= 8 ? 0u : 0x00000001u;
    const u32 tot = __builtin_amdgcn_udot4(cv.x, 0x01010101u, __builtin_amdgcn_udot4(cv.y, 0x01010101u,
                    __builtin_amdgcn_udot4(cv.z, 0x01010101u, 0u, false), false), false);
    const u32 pre = __builtin_amdgcn_udot4(cv.x, m0, __builtin_amdgcn_udot4(cv.y, m1,
                    __builtin_amdgcn_udot4(cv.z, m2, 0u, false), false), false);
    const u32 inc = wave_incl_scan(tot);
    const u32 pk = (inc - tot + pre) | (pre << 10) | (tot > CAPC ? (1u << 20) : 0u);
    if (wv == 0 && tot > CAPC) {
#pragma unroll
      for (int i = 0; i < 8; i++) L.ms[lane * 8 + i] = u32x4{OVF, OVF, OVF, OVF};
    }
    const u32 v = (u32)__builtin_amdgcn_ds_bpermute((int)(hb * 4), (int)pk);
    if (act) {
      const u32 p = (v & 1023u) + r, rank = ((v >> 10) & 1023u) + r;
      L.re[p * 2] = ea0; L.re[p * 2 + 1] = ea1;
      L.rs[p] = shv;
      const bool ok = (v >> 20) == 0;
      L.dest[p] = ok ? ((hb * NCH + (u32)c) * CELLB + rank) : 0xF0000000u;
      if (ok) ((u32*)L.ms)[hb * CAPC + rank] = (u32)(s - (int)hf * HALF) | ((u32)ld << 13);
    }
  }
  __syncthreads();
  TS(10);
  {
    const __amdgpu_buffer_rsrc_t rr = __builtin_amdgcn_make_buffer_rsrc((void*)rec, 0, (int)REC_BYTES, 0x00020000);
    const __amdgpu_buffer_rsrc_t rm = __builtin_amdgcn_make_buffer_rsrc((void*)meta, 0, 64 * CELLS * 4, 0x00020000);
    if (t < 512)
      __builtin_amdgcn_raw_buffer_store_b128(L.ms[t], rm, ((u32)(t >> 3) * CELLS + (u32)c * CAPC) * 4u + (t & 7) * 16u, 0, AUX_K1);
#pragma unroll
    for (int k = 0; k < 2; k++) {
      const int i = t + 640 * k;
      if (i < CH * 2) {
        const u32 dd = L.dest[i >> 1];
        __builtin_amdgcn_raw_buffer_store_b128(L.re[i], rr, (dd & ~31u) + (dd & 31u) * 32u + (u32)(i & 1) * 16u, 0, AUX_K1);
      }
    }
    if (act) {
      const u32 dd = L.dest[t];
      __builtin_amdgcn_raw_buffer_store_b64(L.rs[t], rr, (dd & ~31u) + 1024u + (dd & 31u) * 8u, 0, AUX_K1);
    }
  }
  if (t >= 608) {
    const int item = c * 32 + (t - 608);
    const int tile = item >> 9, ks = (item >> 6) & 7, l = item & 63;
    const int rho = l & 15;
    const int dd = 32 * (tile >> 1) + 8 * (rho >> 2) + 4 * (tile & 1) + (rho & 3);
    const u16* wp = wout + (size_t)(32 * ks + 8 * (l >> 4)) * 256 + dd;
    u32x4 v;
    v.x = (u32)wp[0] | ((u32)wp[256] << 16);
    v.y = (u32)wp[512] | ((u32)wp[768] << 16);
    v.z = (u32)wp[1024] | ((u32)wp[1280] << 16);
    v.w = (u32)wp[1536] | ((u32)wp[1792] << 16);
    wfrag[item] = v;
  }
#pragma unroll
  for (int k = 0; k < PF1; k++) asm volatile("" ::"v"(pf[k]));
  TS(11);
}

constexpr int LCAP = 1280;
constexpr int ZREC = 2048 + LCAP;
struct K2Lds {
  u32x4 ea[2][ZREC + 1];
  u32x2 sh[ZREC + 2];
  u32x4 slot[ITCAP * 2];
  u32x4 stage[136];
  u32x4 cnt[320];
  u32 dh[64];
  u32 wt[8][2][40];
  u16 sorted[320];
  u32 flag;
  u32 wcnt[8];
};

__device__ __forceinline__ u32 slot_index(u32 it, u32 g) { return ((it >> 2) << 5) + (g << 2) + (it & 3); }

#define XOFF(o) (o)
#define POFF(o) (o)
#define K3D 2
#define K3O 0
#define RLA 1
#define RL0 2
#define RL1 4
#define RL2 5
#define RL3 7
#define ORDV 0, 1, 4, 2, 3, 5, 6, 7
#define PFP 3
#define SR0 0
#define SR1 2
#define SR2 5
#define BTOF(k, w) (8 * (k) + ((k) ? 7 - (w) : (w)))
#define SPR 2
constexpr int ORD[8] = {ORDV};
constexpr int NG = 8;

__global__ __launch_bounds__(512, 2) void k2(const u32x4* __restrict__ x, const u32* __restrict__ meta,
                                             const u32x4* __restrict__ rec, const u32x4* __restrict__ wr,
                                             u32x4* __restrict__ part, const int* __restrict__ src,
                                             const int* __restrict__ dst, const u16* __restrict__ sh,
                                             const u16* __restrict__ ea, const u16* __restrict__ cg DBGARG) {
  __shared__ K2Lds L;
  const int t = threadIdx.x, b = blockIdx.x;
  const int lane = t & 63, wv = __builtin_amdgcn_readfirstlane(t >> 6);
  const int sl = (b & 7) >> 1, h = b & 1, bin = b >> 3;
  const int hb = bin * 2 + h;
  const int nnode = (bin == 31) ? (NN - 31 * NB) : NB;
  TS(0);
  u32 md[NG / 2];
  u32x4 m[3];
  u32 pid[3];
  u32x4 R0[NG], R1[NG];
  u32x2 R2[NG];
  u32 ra[NG], rb[NG];
  const __amdgpu_buffer_rsrc_t rr = __builtin_amdgcn_make_buffer_rsrc(
      (void*)((const char*)rec + (size_t)hb * NCH * CELLB), 0, NCH * CELLB, 0x00020000);
  auto ldrec = [&](int k0, int k1) {
    __builtin_amdgcn_sched_barrier(0);
#pragma unroll
    for (int kk = k0; kk < k1; kk++) {
      const int k = ORD[kk];
      R0[k] = __builtin_amdgcn_raw_buffer_load_b128(rr, ra[k], 0, AUX_ML);
      R1[k] = __builtin_amdgcn_raw_buffer_load_b128(rr, ra[k] + 16u, 0, AUX_ML);
      R2[k] = __builtin_amdgcn_raw_buffer_load_b64(rr, rb[k], 0, AUX_ML);
    }
    __builtin_amdgcn_sched_barrier(0);
  };
  {
    const __amdgpu_buffer_rsrc_t rm =
        __builtin_amdgcn_make_buffer_rsrc((void*)(meta + (size_t)hb * CELLS), 0, CELLS * 4, 0x00020000);
#pragma unroll
    for (int k = 0; k < NG / 2; k++) {
      const u32 cell = (u32)(wv * 32 + k * 8 + (lane >> 3));
      md[k] = __builtin_amdgcn_raw_buffer_load_b32(rm, (cell * 32u + (lane & 7)) * 4u, 0, AUX_ML);
      ra[k] = cell * CELLB + (lane & 7) * 32u;
      rb[k] = cell * CELLB + 1024u + (lane & 7) * 8u;
    }
#pragma unroll
    for (int k = 0; k < 3; k++) {
      const u32 i = (u32)(k * 64 + lane), cl = i / 6u;
      pid[k] = ((u32)wv * 32u + cl) * 8u + 2u + (i - cl * 6u);
      m[k] = __builtin_amdgcn_raw_buffer_load_b128(rm, pid[k] * 16u, 0, AUX_ML);
    }
  }
  u32x4 stg = u32x4{0, 0, 0, 0};
  if (t < 128) stg = wr[(t >> 3) * 32 + sl * 8 + (t & 7)];
  else if (t < 136) stg = ((const u32x4*)cg)[t - 128];
  const char* xb = (const char*)x + (size_t)h * HALF * 2048 + sl * 128;
  u32 pf[PF2];
  auto pfx = [&]() {
    __builtin_amdgcn_sched_barrier(0);
#pragma unroll
    for (int k = 0; k < PF2; k++) {
      int i = PFS * PFT + t + 512 * k;
      i = i < 625 * PFT ? i : 625 * PFT - 1;
      pf[k] = *(const u32*)(xb + (u32)(bin * 625 + i / PFT) * 512u + (i % PFT) * PFG);
    }
    __builtin_amdgcn_sched_barrier(0);
  };
  if (PFP == 0) pfx();
  ldrec(0, RLA);
  {
    constexpr u32 PADS = 0xFFFFu | ((u32)ZREC << 16);
    L.slot[t] = u32x4{PADS, PADS, PADS, PADS};
    if (t + 512 < ITCAP * 2) L.slot[t + 512] = u32x4{PADS, PADS, PADS, PADS};
    if (t >= 510) L.ea[t - 510][ZREC] = u32x4{0, 0, 0, 0};
    if (t == 509) L.sh[ZREC] = u32x2{0, 0};
  }
  if (t < 320) L.cnt[t] = u32x4{0, 0, 0, 0};
  else if (t < 384) L.dh[t - 320] = 0;
  else if (t == 384) L.flag = 0;
  ldrec(RLA, RL0);
  if (PFP == 1) pfx();
  __syncthreads();
  u32x4* list = &L.ea[0][2048 + wv * 64];
  u32 np = 0;
#pragma unroll
  for (int k = 0; k < 3; k++) {
    const bool ne = m[k].x != SENT;
    const unsigned long long bal = __ballot(ne);
    u32x4 v = m[k];
    v.x |= (pid[k] & 1023u) << 22;
    v.y |= (pid[k] >> 10) << 22;
    const u32 idx = np + mbcnt64((u32)bal, (u32)(bal >> 32));
    if (ne && idx < NG * 8) list[idx] = v;
    np += (u32)__popcll(bal);
  }
  TS(1);
  const u32 ns = np * 4;
  u32 ev[NG], el[NG], rk[NG];
  const u32 csh = (wv & 1) * 16;
  {
    bool bad = ns > NG * 32;
#pragma unroll
    for (int k = NG / 2; k < NG; k++) {
      const u32 i = (k - NG / 2) * 64 + lane;
      const u32 v = ((const u32*)list)[i];
      const u32 pd = (dpp0<0x00, 0xf, false>(v) >> 22) | ((dpp0<0x55, 0xf, false>(v) >> 22) << 10);
      const u32 ld = (i < ns) ? ((v >> 13) & 511u) : 511u;
      el[k] = ld;
      ev[k] = v & 0x1FFFu;
      const u32 rkc = (pd & 7u) * 4u + (lane & 3);
      const u32 cb = ld < 320 ? (pd >> 3) * CELLB : 0xF0000000u;
      ra[k] = cb + rkc * 32u;
      rb[k] = cb + rkc * 8u + 1024u;
    }
    ldrec(RL0, RL1);
#pragma unroll
    for (int k = 0; k < NG / 2; k++) {
      el[k] = (md[k] >> 13) & 511u;
      ev[k] = md[k] & 0x1FFFu;
      bad |= (el[k] == 510);
    }
#pragma unroll
    for (int k = 0; k < NG; k++) {
      rk[k] = 0;
      if (el[k] < 320) rk[k] = atomicAdd(&((u32*)L.cnt)[el[k] * 4 + (wv >> 1)], 1u << csh);
    }
    if (bad) L.flag = 1;
    if (t < 136) L.stage[t] = stg;
    if (lane == 0) L.wcnt[wv] = ns;
  }
  ldrec(RL1, RL2);
  if (PFP == 2) pfx();
  __syncthreads();
  TS(2);
  u32 lbase;
  {
    const u32 cw = L.wcnt[lane & 7];
    u32 tot = cw;
    tot += dpp0<0xB1, 0xf, false>(tot);
    tot += dpp0<0x4E, 0xf, false>(tot);
    tot += dpp0<0x141, 0xf, false>(tot);
    u32 pw = (lane & 7) < wv ? cw : 0u;
    pw += dpp0<0xB1, 0xf, false>(pw);
    pw += dpp0<0x4E, 0xf, false>(pw);
    pw += dpp0<0x141, 0xf, false>(pw);
    lbase = 2048u + pw + lane;
    if (tot > LCAP) L.flag = 1;
  }
  auto strec = [&](int k0, int k1) {
    __builtin_amdgcn_sched_barrier(0);
#pragma unroll
    for (int kk = k0; kk < k1; kk++) {
      const int k = ORD[kk];
      const u32 ri = k < NG / 2 ? (u32)(wv * 256 + k * 64 + lane) : lbase + (k - NG / 2) * 64;
      if (k < NG / 2 || (el[k] < 320 && ri < ZREC)) {
        L.ea[0][ri] = R0[k];
        L.ea[1][ri] = R1[k];
        L.sh[ri] = R2[k];
      }
    }
    __builtin_amdgcn_sched_barrier(0);
  };
  u32 mydeg = 0, r2 = 0, pre[8];
  if (t < 320) {
    const u32x4 cv = L.cnt[t];
#pragma unroll
    for (int w = 0; w < 8; w++) {
      pre[w] = mydeg;
      mydeg += (cv[w >> 1] >> ((w & 1) * 16)) & 0xFFFFu;
    }
    if (mydeg > 63) { L.flag = 1; mydeg = 63; }
    r2 = atomicAdd(&L.dh[63 - mydeg], 1u);
  }
  ldrec(RL2, RL3);
  if (PFP == 3) pfx();
  strec(0, SR0);
  __syncthreads();
  TS(3);
  u32 hst, binc, blen_l;
  bool slow;
  {
    const u32 hv = L.dh[lane];
    const u32 hinc = wave_incl_scan(hv);
    hst = hinc - hv;
    for (u32 bt = (hst + 7) >> 3; bt < ((hinc + 7) >> 3); bt++) L.wt[wv][0][bt] = (lane == 63) ? 1u : (u32)(63 - lane);
    const int w = lane >> 3, k = lane & 7;
    const int bt = BTOF(k, w) % 40;
    u32 len = L.wt[wv][0][bt];
    if (k >= 5) len = 0;
    u32 tot = len;
    tot += dpp0<0xB1, 0xf, false>(tot);
    tot += dpp0<0x4E, 0xf, false>(tot);
    tot += dpp0<0x141, 0xf, false>(tot);
    if (k == 5) len = (4 - (tot & 3)) & 3;
    binc = wave_incl_scan(len);
    blen_l = len;
    if (k < 5) L.wt[wv][1][bt] = binc - len;
    slow = (L.flag != 0) || ((u32)__builtin_amdgcn_readlane((int)binc, 63) > ITCAP);
  }
  if (!slow) {
    const u32 mypos = (u32)__builtin_amdgcn_ds_bpermute((int)((63 - mydeg) * 4), (int)hst) + r2;
    if (t < 320) {
      const u32 o = L.wt[wv][1][mypos >> 3] | ((mypos & 7) << 10);
      u32x4 pw;
#pragma unroll
      for (int i = 0; i < 4; i++) pw[i] = (o + pre[2 * i]) | ((o + pre[2 * i + 1]) << 16);
      L.cnt[t] = pw;
      L.sorted[mypos] = (u16)t;
    }
    ldrec(RL3, NG);
    strec(SR0, SR1);
    __syncthreads();
#pragma unroll
    for (int k = 0; k < NG; k++) {
      const u32 pw = ((const u16*)L.cnt)[min(el[k], 319u) * 8 + wv];
      rk[k] = slot_index((pw & 1023u) + ((rk[k] >> csh) & 0xFFFFu), pw >> 10);
    }
#pragma unroll
    for (int k = 0; k < NG; k++)
      if (el[k] < 320)
        ((u32*)L.slot)[rk[k]] = ev[k] | ((k < NG / 2 ? (u32)(wv * 256 + k * 64 + lane) : lbase + (k - NG / 2) * 64) << 16);
    const int g = lane >> 3, li = lane & 7;
    const u32 it0 = (u32)__builtin_amdgcn_readlane((int)(binc - blen_l), wv * 8);
    const u32 q0 = it0 >> 2;
    const int nq = (int)(((u32)__builtin_amdgcn_readlane((int)binc, wv * 8 + 5) - it0) >> 2);
    u32x4 Bj[8];
    {
      u32x4 wrow[8];
#pragma unroll
      for (int jj = 0; jj < 8; jj++) wrow[jj] = L.stage[(((lane >> 4) & 1) * 8 + jj) * 8 + (lane & 7)];
      const bool nz = ((lane >> 5) == ((lane >> 3) & 1));
#pragma unroll
      for (int j = 0; j < 8; j++) {
#pragma unroll
        for (int p = 0; p < 4; p++) {
          const u32 lo = wrow[2 * p][j >> 1], hi = wrow[2 * p + 1][j >> 1];
          const u32 v = (j & 1) ? __builtin_amdgcn_perm(hi, lo, 0x07060302u) : __builtin_amdgcn_perm(hi, lo, 0x05040100u);
          Bj[j][p] = nz ? v : 0u;
        }
      }
    }
    u32x2 cgB[4];
    u16 cgr[16];
#pragma unroll
    for (int i = 0; i < 16; i++) cgr[i] = ((const u16*)&L.stage[128])[i * 4 + (lane & 3)];
#pragma unroll
    for (int u = 0; u < 4; u++) {
      cgB[u].x = (u32)cgr[u * 4] | ((u32)cgr[u * 4 + 1] << 16);
      cgB[u].y = (u32)cgr[u * 4 + 2] | ((u32)cgr[u * 4 + 3] << 16);
    }
    strec(SR1, SR2);
    __syncthreads();
    TS(4);
    const __amdgpu_buffer_rsrc_t rx =
        __builtin_amdgcn_make_buffer_rsrc((void*)xb, 0, HALF * 2048 - sl * 128, 0x00020000);
    const u32 loff = li * 16;
    u32x4 X[4][4];
    auto issue_x = [&](int r, u32 s16) {
      const u32 off = s16 * 2048u + loff;
#pragma unroll
      for (int u = 0; u < 4; u++) X[r][u] = __builtin_amdgcn_raw_buffer_load_b128(rx, off + u * 512, 0, 0);
    };
    auto s16of = [&](u32x4 en, int r) { return en[r] & 0xFFFFu; };
    {
      const u32x4 en = L.slot[q0 * 8 + g];
#pragma unroll
      for (int r = 0; r < 4; r++) { issue_x(r, s16of(en, r)); __builtin_amdgcn_sched_barrier(0); }
    }
    TS(7);
    strec(SR2, NG);
    TS(15);
#pragma unroll
    for (int k = 0; k < PF2; k++) asm volatile("" ::"v"(pf[k]));
    __syncthreads();
    TS(6);
    const __amdgpu_buffer_rsrc_t rp = __builtin_amdgcn_make_buffer_rsrc((void*)part, 0, (int)PART_BYTES, 0x00020000);
    const u32 es = (2 * ((lane & 15) >> 2) + (lane >> 5)) * 4 + (lane & 3);
    const u32x4* eap = L.ea[(lane >> 4) & 1];
    const u16* iep = (const u16*)L.slot + (q0 * 32 + es) * 2 + 1;
    const u16* isp = (const u16*)L.slot + (q0 * 32 + g * 4 + (lane & 3)) * 2 + 1;
    const u32x4* srp = L.slot + (q0 + 1) * 8 + g;
    f32x4 acc[8];
#pragma unroll
    for (int j = 0; j < 8; j++) acc[j] = f32x4{0.f, 0.f, 0.f, 0.f};
    u32x4 eav = eap[iep[0]];
    u32x2 shv = L.sh[isp[0]];
    u32 ie = iep[64], is = isp[64];
    u32 it = it0;
    int kb = 0;
    u32 te = (u32)__builtin_amdgcn_readlane((int)binc, wv * 8);
    u32 ndc = L.sorted[wv * 8 + g];
    const u32 pbase = (u32)(h * NN + bin * NB) * 2048u + sl * 128 + li * 16;
#define SB __builtin_amdgcn_sched_barrier(0)
    auto quad = [&](int Q, auto LOADT) {
      constexpr bool LOAD = decltype(LOADT)::value;
      u32x4 en;
      if constexpr (LOAD) en = srp[Q * 8];
      u32x2 av[4];
      f32x4 Du[4], rw[8];
      u32 xo[4];
#pragma unroll
      for (int u = 0; u < 4; u++)
        Du[u] = __builtin_amdgcn_mfma_f32_4x4x4bf16_1k(__builtin_bit_cast(s16x4, shv), __builtin_bit_cast(s16x4, cgB[u]),
                                                       f32x4{0.f, 0.f, 0.f, 0.f}, 0, 0, 0);
      SB;
#pragma unroll
      for (int j = 0; j < 8; j++) {
        rw[j] = __builtin_amdgcn_mfma_f32_16x16x32_bf16(__builtin_bit_cast(bf16x8, eav), __builtin_bit_cast(bf16x8, Bj[j]),
                                                        f32x4{0.f, 0.f, 0.f, 0.f}, 0, 0, 0);
        if (j >= 1 && j <= 4) {
          av[j - 1].x = pack2(Du[0][j - 1], Du[1][j - 1]);
          av[j - 1].y = pack2(Du[2][j - 1], Du[3][j - 1]);
        }
        if constexpr (LOAD) {
          if (j == 5 || j == 6) {
            xo[2 * j - 10] = s16of(en, 2 * j - 10) * 2048u + loff;
            xo[2 * j - 9] = s16of(en, 2 * j - 9) * 2048u + loff;
          }
        }
        SB;
      }
      if constexpr (LOAD) {
        eav = eap[ie];
        shv = L.sh[is];
        ie = iep[(Q + 2) * 64];
        is = isp[(Q + 2) * 64];
      }
#pragma unroll
      for (int r = 0; r < 4; r++) {
        u32x2 Bp[8];
        f32x4 D[8];
        auto prm = [&](int j) {
          const u32 sel = (j & 1) ? 0x07060302u : 0x05040100u;
          Bp[j].x = __builtin_amdgcn_perm(X[r][1][j >> 1], X[r][0][j >> 1], sel);
          Bp[j].y = __builtin_amdgcn_perm(X[r][3][j >> 1], X[r][2][j >> 1], sel);
        };
        auto ldx = [&](int u) { X[r][u] = __builtin_amdgcn_raw_buffer_load_b128(rx, XOFF(xo[r]) + u * 512, 0, 0); };
        prm(0); prm(1);
        SB;
#pragma unroll
        for (int j = 0; j < 8; j++) {
          D[j] = __builtin_amdgcn_mfma_f32_4x4x4bf16_1k(__builtin_bit_cast(s16x4, av[r]), __builtin_bit_cast(s16x4, Bp[j]),
                                                        f32x4{0.f, 0.f, 0.f, 0.f}, 0, 0, 0);
          if (j < 6) prm(j + 2);
          if constexpr (LOAD) {
            if (j == 6) { ldx(0); ldx(1); }
            if (j == 7) { ldx(2); ldx(3); }
          }
          SB;
        }
#pragma unroll
        for (int j = 0; j < 8; j++) acc[j] += D[j] * rw[j][r];
        SB;
        it++;
        if (it == te) {
          const u32 n = ndc < (u32)nnode ? pbase + ndc * 2048u : 0xF0000000u;
#pragma unroll
          for (int w = 0; w < 4; w++) {
            u32x4 o;
            o.x = pack2(acc[0][w], acc[1][w]); o.y = pack2(acc[2][w], acc[3][w]);
            o.z = pack2(acc[4][w], acc[5][w]); o.w = pack2(acc[6][w], acc[7][w]);
            __builtin_amdgcn_raw_buffer_store_b128(o, rp, POFF(n) + w * 512, 0, AUX_K2);
          }
#pragma unroll
          for (int j = 0; j < 8; j++) {
            f32x2 z0, z1;
            asm volatile("v_mov_b64 %0, 0" : "=v"(z0));
            asm volatile("v_mov_b64 %0, 0" : "=v"(z1));
            acc[j] = f32x4{z0.x, z0.y, z1.x, z1.y};
          }
          kb++;
          te = kb < 5 ? (u32)__builtin_amdgcn_readlane((int)binc, wv * 8 + kb) : 0xFFFFFFFFu;
          const int kn = kb < 5 ? kb : 4;
          ndc = L.sorted[BTOF(kn, wv) * 8 + g];
        }
      }
    };
    for (int Q = 0; Q < nq - 1; Q++) quad(Q, std::true_type{});
    quad(nq - 1, std::false_type{});
    return;
  }

  {
    __syncthreads();
    float* accs = (float*)&L.ea[0][0];
    u32* elist = (u32*)&L.ea[0][2048];
    const int w = (t >> 6) & 3, cch = sl * 64 + (t & 63);
    const u16* xs = (const u16*)x;
    const u16* wrs = (const u16*)wr;
    for (int n0 = 0; n0 < nnode; n0 += 32) {
      const int lo = bin * NB + n0;
      const int hi = min(lo + 32, bin * NB + nnode);
      for (int i = t; i < 32 * 256; i += 512) accs[i] = 0.f;
      for (int e0 = 0; e0 < NE; e0 += 512) {
        const int e = e0 + t;
        bool mt = false;
        if (e < NE) {
          const int d = dst[e], s = src[e];
          mt = (d >= lo) && (d < hi) && ((s >= HALF ? 1 : 0) == h);
        }
        const unsigned long long bal = __ballot(mt);
        if (lane == 0) L.wcnt[wv] = (u32)__popcll(bal);
        __syncthreads();
        u32 off = 0, tot = 0;
        for (int i = 0; i < 8; i++) { const u32 cn = L.wcnt[i]; if (i < wv) off += cn; tot += cn; }
        if (mt) elist[off + (u32)__popcll(bal & ((1ull << lane) - 1ull))] = (u32)e;
        __syncthreads();
        if (t < 256) {
          for (u32 i = 0; i < tot; i++) {
            const int ee = (int)elist[i];
            const int s = src[ee], d = dst[ee];
            float rwv = 0.f;
            for (int k = 0; k < 16; k++) rwv += bf1(ea[(size_t)ee * 16 + k]) * bf1(wrs[k * 256 + cch]);
            float tp = 0.f;
            for (int u = 0; u < 4; u++) {
              float a = 0.f;
              for (int v = 0; v < 4; v++) a += bf1(cg[(u * 4 + v) * 4 + w]) * bf1(sh[(size_t)ee * 4 + v]);
              tp += a * bf1(xs[((size_t)s * 4 + u) * 256 + cch]);
            }
            accs[(d - lo) * 256 + t] += tp * rwv;
          }
        }
        __syncthreads();
      }
      if (t < 256) {
        u16* ps = (u16*)part;
        for (int n = lo; n < hi; n++)
          ps[(((size_t)h * NN + n) * 4 + w) * 256 + cch] = (u16)(pack2(accs[(n - lo) * 256 + t], 0.f) & 0xFFFFu);
      }
      __syncthreads();
    }
  }
}

__device__ __forceinline__ u32 addbf2(u32 a, u32 b) { return pack2(bflo(a) + bflo(b), bfhi(a) + bfhi(b)); }
__device__ __forceinline__ u32x4 addbf8(u32x4 a, u32x4 b) {
  u32x4 r;
  r.x = addbf2(a.x, b.x); r.y = addbf2(a.y, b.y); r.z = addbf2(a.z, b.z); r.w = addbf2(a.w, b.w);
  return r;
}

__global__ __launch_bounds__(512, 2) void k3(const u32x4* __restrict__ part, const u32x4* __restrict__ wfrag,
                                             u32x4* __restrict__ out DBGARG) {
  __shared__ u32x4 tl[2][32 * 32];
  const int t = threadIdx.x, b = blockIdx.x;
  const int lane = t & 63, wv = t >> 6;
  const int row = t >> 4, c16 = t & 15;
  TS(12);
  const u32x4* p0 = part + row * 32 + c16;
  const u32x4* p1 = p0 + (size_t)NN * 128;
  const bool five = b < (1250 - 1024);
  u32x4 R[5][4];
  auto ld = [&](int s, int tile) {
    const size_t o = (size_t)tile * 1024;
    R[s][0] = p0[o]; R[s][1] = p0[o + 16]; R[s][2] = p1[o]; R[s][3] = p1[o + 16];
  };
  u32x4 W[2][8];
  auto ldw = [&]() {
    __builtin_amdgcn_sched_barrier(0);
#pragma unroll
    for (int ks = 0; ks < 8; ks++)
#pragma unroll
      for (int p = 0; p < 2; p++) W[p][ks] = wfrag[((wv * 2 + p) * 8 + ks) * 64 + lane];
    __builtin_amdgcn_sched_barrier(0);
  };
  ld(0, b);
  if (K3O == 0) ldw();
  ld(1, b + 256);
  if (K3O == 1) ldw();
  __builtin_amdgcn_sched_barrier(0);
  const __amdgpu_buffer_rsrc_t ro_ = __builtin_amdgcn_make_buffer_rsrc((void*)out, 0, NN * 2048, 0x00020000);
  const int wo = row * 32 + (c16 ^ (row & 15));
  const int ro = (lane & 15) * 32;
  auto step = [&](int i) {
    const int s = i & 1;
    tl[s][wo] = addbf8(R[i][0], R[i][2]);
    tl[s][wo + 16] = addbf8(R[i][1], R[i][3]);
    __builtin_amdgcn_sched_barrier(0);
    if (i + K3D < 4) ld(i + K3D, b + 256 * (i + K3D));
    else if (i + K3D == 4) ld(4, five ? b + 1024 : b);
    __builtin_amdgcn_sched_barrier(0);
    __syncthreads();
    f32x4 acc[2][2];
#pragma unroll
    for (int rt = 0; rt < 2; rt++)
#pragma unroll
      for (int p = 0; p < 2; p++) acc[rt][p] = f32x4{0.f, 0.f, 0.f, 0.f};
#pragma unroll
    for (int ks = 0; ks < 8; ks++) {
      const int pc = ks * 4 + (lane >> 4);
      const int sw = (pc & 16) | ((pc ^ lane) & 15);
#pragma unroll
      for (int rt = 0; rt < 2; rt++) {
        const u32x4 bv = tl[s][rt * 512 + ro + sw];
#pragma unroll
        for (int p = 0; p < 2; p++)
          acc[rt][p] = __builtin_amdgcn_mfma_f32_16x16x32_bf16(__builtin_bit_cast(bf16x8, W[p][ks]),
                                                               __builtin_bit_cast(bf16x8, bv), acc[rt][p], 0, 0, 0);
      }
    }
    const int tile = b + 256 * i;
#pragma unroll
    for (int rt = 0; rt < 2; rt++) {
      u32x4 o;
      o.x = pack2(acc[rt][0][0], acc[rt][0][1]); o.y = pack2(acc[rt][0][2], acc[rt][0][3]);
      o.z = pack2(acc[rt][1][0], acc[rt][1][1]); o.w = pack2(acc[rt][1][2], acc[rt][1][3]);
      __builtin_amdgcn_raw_buffer_store_b128(o, ro_, (u32)(((tile * 32 + rt * 16 + (lane & 15)) * 32 + wv * 4 + (lane >> 4)) * 16), 0, AUX_K3);
    }
  };
  step(0);
  TS(13);
  step(1);
  step(2);
  step(3);
  if (five) step(4);
  TS(14);
}

extern "C" void kernel_launch(void* const* d_in, const int* in_sizes, int n_in, void* d_out, int out_size,
                              void* d_ws, size_t ws_size, hipStream_t stream) {
  const u32x4* x = (const u32x4*)d_in[0];
  const int* src = (const int*)d_in[1];
  const int* dst = (const int*)d_in[2];
  char* ws = (char*)d_ws;
  u32* meta = (u32*)(ws + WS_META);
  u32x4* wfrag = (u32x4*)(ws + WS_WFRAG);
  u32x4* rec = (u32x4*)(ws + WS_REC);
  u32x4* part = (u32x4*)(ws + WS_PART);
#define DBGP
  k1<<<NCH, 640, 0, stream>>>(x, src, dst, (const u32x2*)d_in[3], (const u32x4*)d_in[4], (const u16*)d_in[7], meta,
                              rec, wfrag DBGP);
  k2<<<256, 512, 0, stream>>>(x, meta, rec, (const u32x4*)d_in[6], part, src, dst, (const u16*)d_in[3],
                              (const u16*)d_in[4], (const u16*)d_in[5] DBGP);
  k3<<<256, 512, 0, stream>>>(part, wfrag, (u32x4*)d_out DBGP);
}
```
